```python
import torch

def _softmax(x, axis=-1):
    return torch.softmax(x, dim=axis)
def _gelu(x):
    return torch.nn.functional.gelu(x, approximate="tanh")
def _silu(x):
    return torch.nn.functional.silu(x)
def _sigmoid(x):
    return torch.sigmoid(x)
def _layernorm(x, axis=-1, eps=1e-5):
    m = x.mean(dim=axis, keepdim=True)
    v = x.var(dim=axis, keepdim=True, unbiased=False)
    return (x - m) / torch.sqrt(v + eps)
def _scatter_add(tgt, idx, src, axis=0):
    return tgt.clone().index_add_(axis, idx, src)
def _segment_sum(src, seg, n):
    out = torch.zeros((n,) + src.shape[1:], dtype=src.dtype)
    return out.index_add_(0, seg, src)
def _pad(x, pw):
    flat = tuple(v for pair in reversed(pw) for v in pair)
    return torch.nn.functional.pad(x, flat)

kernel_name = 'sequential_preLN__softmax_full_mha__geglu_ffn__prompt'

def setup_inputs():

    _w = (lambda a: (lambda t: t.to(torch.bfloat16) if t.is_floating_point() else t)(torch.as_tensor(a)))
    def _A_setup():
        B, S, D = 1, 2048, 640
        return {
            "x": _w((lambda *s: torch.randn(*s, dtype=torch.bfloat16) * 0.05)(B, S, D)),
            "wqkv": _w((lambda *s: torch.randn(*s, dtype=torch.bfloat16) * 0.05)(D, 3 * D)),
            "wo": _w((lambda *s: torch.randn(*s, dtype=torch.bfloat16) * 0.05)(D, D)),
        }
    _A_in = _A_setup()

    def _B_setup():
        B, S, D, F = 1, 2048, 640, 1728
        return {
            "x": _w((lambda *s: torch.randn(*s, dtype=torch.bfloat16) * 0.05)(B, S, D)),
            "w_gate": _w((lambda *s: torch.randn(*s, dtype=torch.bfloat16) * 0.05)(D, F)),
            "w_up": _w((lambda *s: torch.randn(*s, dtype=torch.bfloat16) * 0.05)(D, F)),
            "w_down": _w((lambda *s: torch.randn(*s, dtype=torch.bfloat16) * 0.05)(F, D)),
        }
    _B_in = _B_setup()

    return {
        "A_x": _A_in["x"],
        "A_wqkv": _A_in["wqkv"],
        "A_wo": _A_in["wo"],
        "B_x": _B_in["x"],
        "B_w_gate": _B_in["w_gate"],
        "B_w_up": _B_in["w_up"],
        "B_w_down": _B_in["w_down"],
    }

def reference(A_x, A_wqkv, A_wo, B_x, B_w_gate, B_w_up, B_w_down):
    def _A_ref(x, wqkv, wo):
        B, S, D = x.shape
        H = 32; Dh = D // H
        qkv = (lambda x, *d: x.permute(*d))((x @ wqkv).reshape(B, S, 3, H, Dh), 2, 0, 3, 1, 4)
        q, k, v = qkv[0], qkv[1], qkv[2]
        scale = Dh ** -0.5
        scores = torch.einsum("bhqd,bhkd->bhqk", q, k) * scale
        p = _softmax(scores, axis=-1)
        o = torch.einsum("bhqk,bhkd->bhqd", p, v)
        return (lambda x, *d: x.permute(*d))(o, 0, 2, 1, 3).reshape(B, S, D) @ wo

    def _B_ref(x, w_gate, w_up, w_down):
        return (_gelu(x @ w_gate) * (x @ w_up)) @ w_down

    x = A_x
    h = x + _A_ref(_layernorm(x, axis=-1), A_wqkv, A_wo)
    h2 = h + _B_ref(_layernorm(h, axis=-1), B_w_gate, B_w_up, B_w_down)
    return h2
```

```cpp
#include <hip/hip_runtime.h>
#include <hip/hip_bf16.h>
typedef unsigned short u16;
constexpr int S=2048, D=640, H=32, DH=20, F=1728;
__device__ __forceinline__ float bf2f(u16 v){ return __uint_as_float(((unsigned)v)<<16); }
__device__ __forceinline__ u16 f2bf(float f){ __hip_bfloat16 b = __float2bfloat16(f); return *(u16*)&b; }

__global__ void ln_k(const u16* x, u16* y){
  int row=blockIdx.x; int t=threadIdx.x;
  float v[10]; float s=0;
  for(int i=0;i<10;i++){ v[i]=bf2f(x[row*D+t+64*i]); s+=v[i]; }
  for(int o=32;o;o>>=1) s+=__shfl_xor(s,o);
  float m=s/D; float q=0;
  for(int i=0;i<10;i++){ float d=v[i]-m; q+=d*d; }
  for(int o=32;o;o>>=1) q+=__shfl_xor(q,o);
  float r=rsqrtf(q/D+1e-5f);
  for(int i=0;i<10;i++) y[row*D+t+64*i]=f2bf((v[i]-m)*r);
}
__global__ void gemm_k(const u16* A,const u16* B,const u16* res,u16* C,int M,int N,int K){
  __shared__ float As[16][17], Bs[16][17];
  int tx=threadIdx.x&15, ty=threadIdx.x>>4;
  int r=blockIdx.y*16+ty, c=blockIdx.x*16+tx; float acc=0;
  for(int k0=0;k0<K;k0+=16){
    As[ty][tx]=bf2f(A[r*K+k0+tx]); Bs[ty][tx]=bf2f(B[(k0+ty)*N+c]);
    __syncthreads();
    for(int k=0;k<16;k++) acc+=As[ty][k]*Bs[k][tx];
    __syncthreads();
  }
  if(res) acc=bf2f(f2bf(acc))+bf2f(res[r*N+c]);
  C[r*N+c]=f2bf(acc);
}
__global__ void attn_k(const u16* qkv,u16* o){
  int h=blockIdx.y; int q=blockIdx.x*64+threadIdx.x;
  float qv[DH], acc[DH]; for(int d=0;d<DH;d++){ qv[d]=bf2f(qkv[q*3*D+h*DH+d])*0.22360679f; acc[d]=0; }
  float m=-1e30f,l=0;
  for(int k=0;k<S;k++){
    float s=0; for(int d=0;d<DH;d++) s+=qv[d]*bf2f(qkv[k*3*D+D+h*DH+d]);
    float mn=fmaxf(m,s); float a=__expf(m-mn), p=__expf(s-mn); l=l*a+p;
    for(int d=0;d<DH;d++) acc[d]=acc[d]*a+p*bf2f(qkv[k*3*D+2*D+h*DH+d]);
    m=mn;
  }
  for(int d=0;d<DH;d++) o[q*D+h*DH+d]=f2bf(acc[d]/l);
}
__global__ void geglu_k(const u16* g,const u16* u,u16* o,int n){
  int i=blockIdx.x*256+threadIdx.x; if(i>=n) return;
  float x=bf2f(g[i]); float t=tanhf(0.7978845608f*(x+0.044715f*x*x*x));
  o[i]=f2bf(0.5f*x*(1+t)*bf2f(u[i]));
}
extern "C" void kernel_launch(void* const* d_in, const int* in_sizes, int n_in, void* d_out, int out_size, void* d_ws, size_t ws_size, hipStream_t stream) {
  const u16* x=(const u16*)d_in[0]; const u16* wqkv=(const u16*)d_in[1]; const u16* wo=(const u16*)d_in[2];
  const u16* wg=(const u16*)d_in[4]; const u16* wu=(const u16*)d_in[5]; const u16* wd=(const u16*)d_in[6];
  u16* ws=(u16*)d_ws; u16* xn=ws; u16* qkv=xn+S*D; u16* o=qkv+S*3*D; u16* h=o+S*D; u16* hn=h+S*D; u16* g=hn+S*D; u16* u=g+S*F; u16* a=u+S*F;
  ln_k<<<S,64,0,stream>>>(x,xn);
  gemm_k<<<dim3(3*D/16,S/16),256,0,stream>>>(xn,wqkv,nullptr,qkv,S,3*D,D);
  attn_k<<<dim3(S/64,H),64,0,stream>>>(qkv,o);
  gemm_k<<<dim3(D/16,S/16),256,0,stream>>>(o,wo,x,h,S,D,D);
  ln_k<<<S,64,0,stream>>>(h,hn);
  gemm_k<<<dim3(F/16,S/16),256,0,stream>>>(hn,wg,nullptr,g,S,F,D);
  gemm_k<<<dim3(F/16,S/16),256,0,stream>>>(hn,wu,nullptr,u,S,F,D);
  geglu_k<<<(S*F+255)/256,256,0,stream>>>(g,u,a,S*F);
  gemm_k<<<dim3(D/16,S/16),256,0,stream>>>(a,wd,h,(u16*)d_out,S,D,F);
}
```

```cpp
#include <hip/hip_runtime.h>
#include <hip/hip_bf16.h>
#include <stdint.h>

typedef unsigned short u16;
typedef __bf16 bf16x2 __attribute__((ext_vector_type(2)));
typedef __bf16 bf16x8 __attribute__((ext_vector_type(8)));
typedef short s16x4 __attribute__((ext_vector_type(4)));
typedef short s16x8 __attribute__((ext_vector_type(8)));
typedef float f32x2 __attribute__((ext_vector_type(2)));
typedef float f32x4 __attribute__((ext_vector_type(4)));
typedef float f32x16 __attribute__((ext_vector_type(16)));
typedef unsigned u32x2 __attribute__((ext_vector_type(2)));
typedef unsigned u32x4 __attribute__((ext_vector_type(4)));
#define LAS __attribute__((address_space(3)))
#define DI __device__ __forceinline__

#define ATTN_NW 8
constexpr int S = 2048, D = 640, NH = 32, DH = 20, FF = 1728;
constexpr int QK_TILE = 1280;
constexpr int V_TILE = 1344;
constexpr size_t WS_Q = 0;
constexpr size_t WS_K = WS_Q + (size_t)NH * 64 * QK_TILE;
constexpr size_t WS_V = WS_K + (size_t)NH * 64 * QK_TILE;
constexpr size_t WS_O = WS_V + (size_t)NH * 64 * V_TILE;
constexpr size_t WS_H = WS_O + (size_t)64 * 40 * 1024;
constexpr size_t WS_G = WS_H + (size_t)64 * 40 * 1024;
constexpr size_t WS_END = WS_G + (size_t)64 * 108 * 1024;
constexpr size_t WS_TS = 32u << 20;
constexpr float LN_EPS = 1e-5f;
constexpr float QSCALE = 0.22360679774997896f * 1.4426950408889634f;

#define TS_BEGIN(kid)
#define TS_END(kid, wsp)

DI unsigned pk2(float a, float b) { f32x2 p = {a, b}; return __builtin_bit_cast(unsigned, __builtin_convertvector(p, bf16x2)); }
DI float dot2(unsigned a, unsigned b, float c) { return __builtin_amdgcn_fdot2_f32_bf16(__builtin_bit_cast(bf16x2, a), __builtin_bit_cast(bf16x2, b), c, false); }
DI f32x16 mfma32(u32x4 a, u32x4 b, f32x16 c) { return __builtin_amdgcn_mfma_f32_32x32x16_bf16(__builtin_bit_cast(bf16x8, a), __builtin_bit_cast(bf16x8, b), c, 0, 0, 0); }
DI f32x16 mfma32k8(u32x2 a, u32x2 b, f32x16 c) { return __builtin_amdgcn_mfma_f32_32x32x8bf16_1k(__builtin_bit_cast(s16x4, a), __builtin_bit_cast(s16x4, b), c, 0, 0, 0); }
DI f32x4 mfma16(u32x4 a, u32x4 b, f32x4 c) { return __builtin_amdgcn_mfma_f32_16x16x32_bf16(__builtin_bit_cast(bf16x8, a), __builtin_bit_cast(bf16x8, b), c, 0, 0, 0); }
DI u32x2 trread(LAS const char* p) { return __builtin_bit_cast(u32x2, __builtin_amdgcn_ds_read_tr16_b64_v4i16((LAS s16x4*)p)); }
DI u32x4 trfrag(LAS const char* p0, LAS const char* p1) { u32x2 a = trread(p0), b = trread(p1); return (u32x4){a[0], a[1], b[0], b[1]}; }
DI float swap32(float v) {
    auto r = __builtin_amdgcn_permlane32_swap(__float_as_uint(v), __float_as_uint(v), false, false);
    return (threadIdx.x & 32) ? __uint_as_float(r[0]) : __uint_as_float(r[1]);
}
DI float bf_lo(unsigned w) { return __uint_as_float(w << 16); }
DI float bf_hi(unsigned w) { return __uint_as_float(w & 0xffff0000u); }
#define WAIT_VM(n) asm volatile("s_waitcnt vmcnt(" #n ")" ::: "memory")
template <int N> DI void wait_vm() { asm volatile("s_waitcnt vmcnt(%0)" :: "i"(N) : "memory"); }
template <int N> DI void wait_vm_bar() { asm volatile("s_waitcnt vmcnt(%0) lgkmcnt(0)\n\ts_barrier" :: "i"(N) : "memory"); }
template <int OFF> DI void glds(const void* gbase, unsigned voff, unsigned lds) {
    unsigned keep;
    asm volatile("s_mov_b32 %0, m0\n\ts_mov_b32 m0, %3\n\ts_nop 0\n\tglobal_load_lds_dwordx4 %1, %2 offset:%4\n\ts_mov_b32 m0, %0"
                 : "=&s"(keep) : "v"(voff), "s"(gbase), "s"(lds), "i"(OFF) : "memory");
}
DI void glds4(const void* gbase, unsigned v0, unsigned v1, unsigned v2, unsigned v3, unsigned lds) {
    unsigned keep;
    asm volatile("s_mov_b32 %0, m0\n\ts_mov_b32 m0, %6\n\ts_nop 0\n\t"
                 "global_load_lds_dwordx4 %1, %5\n\tglobal_load_lds_dwordx4 %2, %5 offset:1024\n\t"
                 "global_load_lds_dwordx4 %3, %5 offset:2048\n\tglobal_load_lds_dwordx4 %4, %5 offset:3072\n\ts_mov_b32 m0, %0"
                 : "=&s"(keep) : "v"(v0), "v"(v1), "v"(v2), "v"(v3), "s"(gbase), "s"(lds) : "memory");
}
DI void glds3(const void* gbase, unsigned v0, unsigned v1, unsigned v2, unsigned lds) {
    unsigned keep;
    asm volatile("s_mov_b32 %0, m0\n\ts_mov_b32 m0, %5\n\ts_nop 0\n\t"
                 "global_load_lds_dwordx4 %1, %4\n\tglobal_load_lds_dwordx4 %2, %4 offset:1024\n\t"
                 "global_load_lds_dwordx4 %3, %4 offset:2048\n\ts_mov_b32 m0, %0"
                 : "=&s"(keep) : "v"(v0), "v"(v1), "v"(v2), "s"(gbase), "s"(lds) : "memory");
}
template <int OFF> DI void gload16(u32x4& dst, const void* gbase, unsigned voff) {
    asm volatile("global_load_dwordx4 %0, %1, %2 offset:%3" : "=v"(dst) : "v"(voff), "s"(gbase), "i"(OFF) : "memory");
}
#define PIN(x) asm volatile("" : "+v"(x))
#define SBAR() __builtin_amdgcn_sched_barrier(0)
template <int I> struct IC { static constexpr int v = I; };
template <int I, int N, class F> DI void sfor_(F& f) { if constexpr (I < N) { f(IC<I>{}); sfor_<I + 1, N>(f); } }
template <int N, class F> DI void sfor(F f) { sfor_<0, N>(f); }
template <int N> DI void wait_vm_bar_nl() { asm volatile("s_waitcnt vmcnt(%0)\n\ts_barrier" :: "i"(N) : "memory"); }
DI int opq_add(int v, int k) { int r; asm("v_add_u32 %0, %1, %2" : "=v"(r) : "s"(k), "v"(v)); return r; }
#define OPQ(v, hi) ((hi) ? opq_add((v), (hi)) : (v))

template <bool ISV>
DI void qkv_body(const u16* x, const u16* w, char* ws, int pm, int pn, char* smem) {
    const int tid = threadIdx.x, lane = tid & 63, wid = __builtin_amdgcn_readfirstlane(tid >> 6);
    const int wr = wid >> 1, wc = wid & 1, r32 = lane & 31, hf = lane >> 5;
    const unsigned lds0 = (unsigned)(uintptr_t)(LAS char*)smem;
    unsigned xo[4], wo_[4];
#pragma unroll
    for (int i = 0; i < 4; ++i) {
        const int row = (wid * 4 + i) * 8 + (lane >> 3), cp = lane & 7, c = cp ^ ((row >> 1) & 7);
        xo[i] = (unsigned)(row * (D * 2) + c * 16 + 4096 - i * 1024);
        const int kq = wid * 4 + i, nb = lane >> 4, q = (lane >> 2) & 3, c4 = lane & 3;
        wo_[i] = (unsigned)((kq * 4 + q) * (3 * D * 2) + nb * 64 + c4 * 16 + 4096 - i * 1024);
    }
    const char* xb = (const char*)x + (size_t)pm * 128 * D * 2 - 4096;
    const char* wb = (const char*)w + pn * 256 - 4096;
    auto issue_x = [&](int c) { glds4(xb + c * 128, xo[0], xo[1], xo[2], xo[3], lds0 + (c % 5) * 32768 + wid * 4096); };
    auto issue_w = [&](int c) { glds4(wb + (size_t)c * 64 * (3 * D * 2), wo_[0], wo_[1], wo_[2], wo_[3], lds0 + (c % 5) * 32768 + wid * 4096 + 16384); };
#pragma unroll
    for (int c = 0; c < 5; ++c) { issue_x(c); issue_w(c); }
    LAS const char* sm = (LAS const char*)smem;
    int xa[4];
#pragma unroll
    for (int s = 0; s < 4; ++s) xa[s] = r32 * 128 + (((2 * s + hf) ^ ((r32 >> 1) & 7)) << 4);
    const int tb0 = 2 * wr + wc, tb1 = 2 * wr + (wc ^ 1), cb0 = 2 * wc + wr, cb1 = 2 * wc + (wr ^ 1);
    const int wl = 16384 + hf * 2048 + ((lane >> 2) & 3) * 64 + ((lane >> 4) & 1) * 32 + (lane & 3) * 8;
    f32x16 acc[2][2] = {};
    float sx = 0.f, sxx = 0.f, sw = 0.f;
    u32x4 fx[2][2], fw[2][2];
    int xs0[4], xs1[4], w0a = wl + cb0 * 256, w1a = wl + cb1 * 256;
#pragma unroll
    for (int s = 0; s < 4; ++s) { xs0[s] = xa[s] + tb0 * 4096; xs1[s] = xa[s] + tb1 * 4096; }
    auto rd = [&](int st, int which) {
        const int c = st >> 2, s = st & 3, so = (c % 5) * 32768, hi = so >= 65536 ? (so >= 131072 ? 131072 : 65536) : 0;
        LAS const char* p = sm + (so - hi);
        if (which == 0) fx[st & 1][0] = *(LAS const u32x4*)(p + OPQ(xs0[s], hi));
        if (which == 1) fw[st & 1][0] = trfrag(p + OPQ(w0a, hi) + s * 4096, p + OPQ(w0a, hi) + s * 4096 + 1024);
        if (which == 2) fw[st & 1][1] = trfrag(p + OPQ(w1a, hi) + s * 4096, p + OPQ(w1a, hi) + s * 4096 + 1024);
        if (which == 3) fx[st & 1][1] = *(LAS const u32x4*)(p + OPQ(xs1[s], hi));
    };
    wait_vm_bar<32>();
    rd(0, 0); rd(0, 1); rd(0, 2); rd(0, 3);
    SBAR();
#pragma unroll
    for (int st = 0; st < 40; ++st) {
        const int c = st >> 2, s = st & 3, bf = st & 1;
        if (s == 3 && c < 9) {
            if (c == 0) wait_vm_bar_nl<24>(); else if (c <= 5) wait_vm_bar_nl<16>(); else if (c == 6) wait_vm_bar_nl<16>(); else if (c == 7) wait_vm_bar_nl<8>(); else wait_vm_bar_nl<0>();
        }
        const u32x4 x0 = fx[bf][0], x1 = fx[bf][1], w0 = fw[bf][0], w1 = fw[bf][1];
        if (st + 1 < 40) rd(st + 1, 0);
        acc[0][0] = ISV ? mfma32(x0, w0, acc[0][0]) : mfma32(w0, x0, acc[0][0]);
        sx = dot2(x0[0], 0x3f803f80u, sx); sxx = dot2(x0[0], x0[0], sxx); sw = dot2(w0[0], 0x3f803f80u, sw);
        SBAR();
        if (st + 1 < 40) rd(st + 1, 1);
        acc[0][1] = ISV ? mfma32(x0, w1, acc[0][1]) : mfma32(w1, x0, acc[0][1]);
        sx = dot2(x0[1], 0x3f803f80u, sx); sxx = dot2(x0[1], x0[1], sxx); sw = dot2(w0[1], 0x3f803f80u, sw);
        if (s == 3 && c >= 1 && c + 4 < 10) issue_x(c + 4);
        SBAR();
        if (st + 1 < 40) rd(st + 1, 2);
        acc[1][0] = ISV ? mfma32(x1, w0, acc[1][0]) : mfma32(w0, x1, acc[1][0]);
        sx = dot2(x0[2], 0x3f803f80u, sx); sxx = dot2(x0[2], x0[2], sxx); sw = dot2(w0[2], 0x3f803f80u, sw);
        if (s == 3 && c >= 1 && c + 4 < 10) issue_w(c + 4);
        SBAR();
        if (st + 1 < 40) rd(st + 1, 3);
        acc[1][1] = ISV ? mfma32(x1, w1, acc[1][1]) : mfma32(w1, x1, acc[1][1]);
        sx = dot2(x0[3], 0x3f803f80u, sx); sxx = dot2(x0[3], x0[3], sxx); sw = dot2(w0[3], 0x3f803f80u, sw);
        SBAR();
    }
    sx += swap32(sx); sxx += swap32(sxx); sw += swap32(sw);
    const float mean = sx * (1.f / D), var = fmaxf(sxx * (1.f / D) - mean * mean, 0.f), rstd = __builtin_amdgcn_rsqf(var + LN_EPS);
    LAS float* st = (LAS float*)smem;
    if (hf == 0) { st[tb0 * 32 + r32] = rstd; st[128 + tb0 * 32 + r32] = -mean; st[256 + cb0 * 32 + r32] = sw; }
    asm volatile("s_waitcnt lgkmcnt(0)\n\ts_barrier" ::: "memory");
    if (ISV) {
#pragma unroll
        for (int j = 0; j < 2; ++j) {
            const int cb = j ? cb1 : cb0, col = (pn - 10) * 128 + cb * 32 + r32, head = col / DH, d = col - head * DH;
            const float cs = st[256 + cb * 32 + r32];
#pragma unroll
            for (int i = 0; i < 2; ++i) {
                const int tb = i ? tb1 : tb0;
                char* dst = ws + WS_V + (size_t)(head * 64 + pm * 4 + tb) * V_TILE + hf * 336 + d * 16;
                unsigned o[8];
#pragma unroll
                for (int g = 0; g < 4; ++g) {
                    const f32x4 rs = *(LAS const f32x4*)(st + tb * 32 + 8 * g + 4 * hf), nm = *(LAS const f32x4*)(st + 128 + tb * 32 + 8 * g + 4 * hf);
                    float v[4];
#pragma unroll
                    for (int e = 0; e < 4; ++e) v[e] = rs[e] * fmaf(nm[e], cs, acc[i][j][4 * g + e]);
                    o[2 * g] = pk2(v[0], v[1]); o[2 * g + 1] = pk2(v[2], v[3]);
                }
                *(u32x4*)(dst) = (u32x4){o[0], o[1], o[2], o[3]};
                *(u32x4*)(dst + 672) = (u32x4){o[4], o[5], o[6], o[7]};
                if (d == DH - 1) { const u32x4 one = {0x3f803f80u, 0x3f803f80u, 0x3f803f80u, 0x3f803f80u}; *(u32x4*)(dst + 16) = one; *(u32x4*)(dst + 672 + 16) = one; }
            }
        }
    } else {
        const bool isq = pn < 5;
        char* base = ws + (isq ? WS_Q : WS_K);
        const int cq0 = (isq ? pn : pn - 5) * 32;
#pragma unroll
        for (int i = 0; i < 2; ++i) {
            const int tb = i ? tb1 : tb0;
            float rs = st[tb * 32 + r32]; const float nm = st[128 + tb * 32 + r32];
            if (isq) rs *= QSCALE;
#pragma unroll
            for (int j = 0; j < 2; ++j) {
                const int cb = j ? cb1 : cb0;
#pragma unroll
                for (int g = 0; g < 4; ++g) {
                    const f32x4 cs = *(LAS const f32x4*)(st + 256 + cb * 32 + 8 * g + 4 * hf);
                    float v[4];
#pragma unroll
                    for (int e = 0; e < 4; ++e) v[e] = rs * fmaf(nm, cs[e], acc[i][j][4 * g + e]);
                    const int cq = cq0 + cb * 8 + 2 * g + hf, head = cq / 5, qd = cq - head * 5;
                    const int off = qd < 4 ? (qd >> 1) * 512 + r32 * 16 + (qd & 1) * 8 : 1024 + r32 * 8;
                    *(u32x2*)(base + (size_t)(head * 64 + pm * 4 + tb) * QK_TILE + off) = (u32x2){pk2(v[0], v[1]), pk2(v[2], v[3])};
                }
            }
        }
    }
}
__global__ __launch_bounds__(256, 1) void qkv_kernel(const u16* __restrict__ x, const u16* __restrict__ w, char* ws) {
    extern __shared__ __attribute__((aligned(16))) char smem[];
    TS_BEGIN(0)
    const int xcd = blockIdx.x & 7, j = (xcd & 1) * 30 + (blockIdx.x >> 3), pn = j >> 2, pm = (xcd >> 1) * 4 + (j & 3);
    if (pn >= 10) qkv_body<true>(x, w, ws, pm, pn, smem); else qkv_body<false>(x, w, ws, pm, pn, smem);
    TS_END(0, ws);
}

struct KV { u32x4 k0; u32x2 k1; u32x4 v0, v1; };
DI void kv_load(KV& t, const char* kp, const char* vp, int ko0, int ko1, int vo) {
    t.k0 = *(const u32x4*)(kp + ko0); t.k1 = *(const u32x2*)(kp + ko1);
    t.v0 = *(const u32x4*)(vp + vo); t.v1 = *(const u32x4*)(vp + vo + 672);
}
DI void exp_pack(const f32x16& s, u32x4& p0, u32x4& p1) {
#pragma unroll
    for (int i = 0; i < 4; ++i) {
        p0[i] = pk2(__builtin_amdgcn_exp2f(s[2 * i]), __builtin_amdgcn_exp2f(s[2 * i + 1]));
        p1[i] = pk2(__builtin_amdgcn_exp2f(s[8 + 2 * i]), __builtin_amdgcn_exp2f(s[8 + 2 * i + 1]));
    }
}
DI float max16(const f32x16& s) {
    float m = fmaxf(fmaxf(s[0], s[1]), s[2]);
#pragma unroll
    for (int i = 3; i < 15; i += 2) m = fmaxf(fmaxf(m, s[i]), s[i + 1]);
    m = fmaxf(m, s[15]);
    return fmaxf(m, swap32(m));
}
DI void store_o(char* ws, int T, int head, int lane, const float* o, float inv) {
    char* ob = ws + WS_O + (size_t)T * 40 * 1024;
    *(u32x4*)(ob + head * 1024 + lane * 16) = (u32x4){pk2(o[0] * inv, o[1] * inv), pk2(o[2] * inv, o[3] * inv), pk2(o[4] * inv, o[5] * inv), pk2(o[6] * inv, o[7] * inv)};
    if (lane < 32) *(u32x2*)(ob + (32 + (head >> 2)) * 1024 + (head & 1) * 512 + lane * 16 + ((head >> 1) & 1) * 8) = (u32x2){pk2(o[8] * inv, o[9] * inv), pk2(o[10] * inv, o[11] * inv)};
}
#define ATTN_ASM_MAIN \
    "v_mov_b32 v120, 0\n\t" \
    "v_mov_b32 v121, 0\n\t" \
    "v_mov_b32 v122, 0\n\t" \
    "v_mov_b32 v123, 0\n\t" \
    "s_mov_b32 exec_hi, 0\n\t" \
    "global_load_dwordx2 v[120:121], v167, s[44:45]\n\t" \
    "global_load_dwordx2 v[122:123], v167, s[44:45] offset:1280\n\t" \
    "s_mov_b32 exec_hi, -1\n\t" \
    "global_load_dwordx2 v[164:165], v167, s[46:47]\n\t" \
    "global_load_dwordx2 v[108:109], v167, s[40:41] offset:-1280\n\t" \
    "global_load_dwordx4 v[112:115], v166, s[44:45]\n\t" \
    "global_load_dwordx4 v[116:119], v166, s[44:45] offset:1280\n\t" \
    "global_load_dwordx4 v[152:155], v166, s[46:47]\n\t" \
    "global_load_dwordx4 v[104:107], v166, s[40:41] offset:-1280\n\t" \
    "global_load_dwordx4 v[124:127], v166, s[40:41] offset:0\n\t" \
    "global_load_dwordx2 v[136:137], v167, s[40:41] offset:0\n\t" \
    "global_load_dwordx4 v[128:131], v168, s[42:43] offset:0\n\t" \
    "global_load_dwordx4 v[132:135], v168, s[42:43] offset:672\n\t" \
    "global_load_dwordx4 v[138:141], v166, s[40:41] offset:1280\n\t" \
    "global_load_dwordx2 v[150:151], v167, s[40:41] offset:1280\n\t" \
    "global_load_dwordx4 v[142:145], v168, s[42:43] offset:1344\n\t" \
    "global_load_dwordx4 v[146:149], v168, s[42:43] offset:2016\n\t" \
    "s_add_u32 s40, s40, 2560\n\t" \
    "s_addc_u32 s41, s41, 0\n\t" \
    "s_add_u32 s42, s42, 2688\n\t" \
    "s_addc_u32 s43, s43, 0\n\t" \
    "v_mov_b32 v0, 0\n\t" \
    "v_mov_b32 v1, 0\n\t" \
    "v_mov_b32 v2, 0\n\t" \
    "v_mov_b32 v3, 0\n\t" \
    "v_mov_b32 v4, 0\n\t" \
    "v_mov_b32 v5, 0\n\t" \
    "v_mov_b32 v6, 0\n\t" \
    "v_mov_b32 v7, 0\n\t" \
    "v_mov_b32 v8, 0\n\t" \
    "v_mov_b32 v9, 0\n\t" \
    "v_mov_b32 v10, 0\n\t" \
    "v_mov_b32 v11, 0\n\t" \
    "v_mov_b32 v12, 0\n\t" \
    "v_mov_b32 v13, 0\n\t" \
    "v_mov_b32 v14, 0\n\t" \
    "v_mov_b32 v15, 0\n\t" \
    "v_mov_b32 v16, 0\n\t" \
    "v_mov_b32 v17, 0\n\t" \
    "v_mov_b32 v18, 0\n\t" \
    "v_mov_b32 v19, 0\n\t" \
    "v_mov_b32 v20, 0\n\t" \
    "v_mov_b32 v21, 0\n\t" \
    "v_mov_b32 v22, 0\n\t" \
    "v_mov_b32 v23, 0\n\t" \
    "v_mov_b32 v24, 0\n\t" \
    "v_mov_b32 v25, 0\n\t" \
    "v_mov_b32 v26, 0\n\t" \
    "v_mov_b32 v27, 0\n\t" \
    "v_mov_b32 v28, 0\n\t" \
    "v_mov_b32 v29, 0\n\t" \
    "v_mov_b32 v30, 0\n\t" \
    "v_mov_b32 v31, 0\n\t" \
    "s_waitcnt vmcnt(9)\n\t" \
    "v_mfma_f32_32x32x16_bf16 v[64:79], v[152:155], v[112:115], 0\n\t" \
    "v_mfma_f32_32x32x8_bf16 v[64:79], v[164:165], v[120:121], v[64:79]\n\t" \
    "v_mfma_f32_32x32x16_bf16 v[80:95], v[152:155], v[116:119], 0\n\t" \
    "v_mfma_f32_32x32x8_bf16 v[80:95], v[164:165], v[122:123], v[80:95]\n\t" \
    "s_nop 15\n\t" \
    "v_max3_f32 v32, v64, v65, v66\n\t" \
    "v_max3_f32 v32, v32, v67, v68\n\t" \
    "v_max3_f32 v32, v32, v69, v70\n\t" \
    "v_max3_f32 v32, v32, v71, v72\n\t" \
    "v_max3_f32 v32, v32, v73, v74\n\t" \
    "v_max3_f32 v32, v32, v75, v76\n\t" \
    "v_max3_f32 v32, v32, v77, v78\n\t" \
    "v_max_f32 v32, v32, v79\n\t" \
    "v_mov_b32 v33, v32\n\t" \
    "s_nop 1\n\t" \
    "v_permlane32_swap_b32 v32, v33\n\t" \
    "s_nop 1\n\t" \
    "v_max_f32 v32, v32, v33\n\t" \
    "v_sub_f32 v32, 0, v32\n\t" \
    "v_mov_b32 v33, v32\n\t" \
    "v_mov_b32 v34, v32\n\t" \
    "v_mov_b32 v35, v32\n\t" \
    "v_mov_b32 v36, v32\n\t" \
    "v_mov_b32 v37, v32\n\t" \
    "v_mov_b32 v38, v32\n\t" \
    "v_mov_b32 v39, v32\n\t" \
    "v_mov_b32 v40, v32\n\t" \
    "v_mov_b32 v41, v32\n\t" \
    "v_mov_b32 v42, v32\n\t" \
    "v_mov_b32 v43, v32\n\t" \
    "v_mov_b32 v44, v32\n\t" \
    "v_mov_b32 v45, v32\n\t" \
    "v_mov_b32 v46, v32\n\t" \
    "v_mov_b32 v47, v32\n\t" \
    "v_max3_f32 v48, v80, v81, v82\n\t" \
    "v_max3_f32 v48, v48, v83, v84\n\t" \
    "v_max3_f32 v48, v48, v85, v86\n\t" \
    "v_max3_f32 v48, v48, v87, v88\n\t" \
    "v_max3_f32 v48, v48, v89, v90\n\t" \
    "v_max3_f32 v48, v48, v91, v92\n\t" \
    "v_max3_f32 v48, v48, v93, v94\n\t" \
    "v_max_f32 v48, v48, v95\n\t" \
    "v_mov_b32 v49, v48\n\t" \
    "s_nop 1\n\t" \
    "v_permlane32_swap_b32 v48, v49\n\t" \
    "s_nop 1\n\t" \
    "v_max_f32 v48, v48, v49\n\t" \
    "v_sub_f32 v48, 0, v48\n\t" \
    "v_mov_b32 v49, v48\n\t" \
    "v_mov_b32 v50, v48\n\t" \
    "v_mov_b32 v51, v48\n\t" \
    "v_mov_b32 v52, v48\n\t" \
    "v_mov_b32 v53, v48\n\t" \
    "v_mov_b32 v54, v48\n\t" \
    "v_mov_b32 v55, v48\n\t" \
    "v_mov_b32 v56, v48\n\t" \
    "v_mov_b32 v57, v48\n\t" \
    "v_mov_b32 v58, v48\n\t" \
    "v_mov_b32 v59, v48\n\t" \
    "v_mov_b32 v60, v48\n\t" \
    "v_mov_b32 v61, v48\n\t" \
    "v_mov_b32 v62, v48\n\t" \
    "v_mov_b32 v63, v48\n\t" \
    "s_waitcnt vmcnt(8)\n\t" \
    "s_nop 1\n\t" \
    "v_mfma_f32_32x32x16_bf16 v[64:79], v[104:107], v[112:115], v[32:47]\n\t" \
    "v_mfma_f32_32x32x8_bf16 v[64:79], v[108:109], v[120:121], v[64:79]\n\t" \
    "v_mfma_f32_32x32x16_bf16 v[80:95], v[104:107], v[116:119], v[48:63]\n\t" \
    "v_mfma_f32_32x32x8_bf16 v[80:95], v[108:109], v[122:123], v[80:95]\n\t" \
    "s_nop 11\n\t" \
    "v_exp_f32 v64, v64\n\t" \
    "v_exp_f32 v65, v65\n\t" \
    "v_exp_f32 v66, v66\n\t" \
    "v_exp_f32 v67, v67\n\t" \
    "v_cvt_pk_bf16_f32 v96, v64, v65\n\t" \
    "v_exp_f32 v68, v68\n\t" \
    "v_exp_f32 v69, v69\n\t" \
    "v_cvt_pk_bf16_f32 v97, v66, v67\n\t" \
    "v_exp_f32 v70, v70\n\t" \
    "v_exp_f32 v71, v71\n\t" \
    "v_cvt_pk_bf16_f32 v98, v68, v69\n\t" \
    "v_exp_f32 v72, v72\n\t" \
    "v_exp_f32 v73, v73\n\t" \
    "v_cvt_pk_bf16_f32 v99, v70, v71\n\t" \
    "v_exp_f32 v74, v74\n\t" \
    "v_exp_f32 v75, v75\n\t" \
    "v_cvt_pk_bf16_f32 v100, v72, v73\n\t" \
    "v_exp_f32 v76, v76\n\t" \
    "v_exp_f32 v77, v77\n\t" \
    "v_cvt_pk_bf16_f32 v101, v74, v75\n\t" \
    "v_exp_f32 v78, v78\n\t" \
    "v_exp_f32 v79, v79\n\t" \
    "v_cvt_pk_bf16_f32 v102, v76, v77\n\t" \
    "v_cvt_pk_bf16_f32 v103, v78, v79\n\t" \
    "1:\n\t" \
    "v_mfma_f32_32x32x16_bf16 v[80:95], v[124:127], v[116:119], v[48:63]\n\t" \
    "v_exp_f32 v64, v64\n\t" \
    "v_exp_f32 v65, v65\n\t" \
    "v_exp_f32 v66, v66\n\t" \
    "v_exp_f32 v67, v67\n\t" \
    "v_cvt_pk_bf16_f32 v96, v64, v65\n\t" \
    "v_exp_f32 v68, v68\n\t" \
    "v_mfma_f32_32x32x8_bf16 v[80:95], v[136:137], v[122:123], v[80:95]\n\t" \
    "v_exp_f32 v69, v69\n\t" \
    "v_cvt_pk_bf16_f32 v97, v66, v67\n\t" \
    "v_exp_f32 v70, v70\n\t" \
    "v_exp_f32 v71, v71\n\t" \
    "v_cvt_pk_bf16_f32 v98, v68, v69\n\t" \
    "v_exp_f32 v72, v72\n\t" \
    "v_mfma_f32_32x32x16_bf16 v[16:31], v[128:131], v[104:107], v[16:31]\n\t" \
    "v_exp_f32 v73, v73\n\t" \
    "v_cvt_pk_bf16_f32 v99, v70, v71\n\t" \
    "v_exp_f32 v74, v74\n\t" \
    "v_exp_f32 v75, v75\n\t" \
    "v_cvt_pk_bf16_f32 v100, v72, v73\n\t" \
    "v_exp_f32 v76, v76\n\t" \
    "v_mfma_f32_32x32x16_bf16 v[16:31], v[132:135], v[108:111], v[16:31]\n\t" \
    "v_exp_f32 v77, v77\n\t" \
    "v_cvt_pk_bf16_f32 v101, v74, v75\n\t" \
    "v_exp_f32 v78, v78\n\t" \
    "v_exp_f32 v79, v79\n\t" \
    "v_cvt_pk_bf16_f32 v102, v76, v77\n\t" \
    "v_cvt_pk_bf16_f32 v103, v78, v79\n\t" \
    "v_mfma_f32_32x32x16_bf16 v[64:79], v[124:127], v[112:115], v[32:47]\n\t" \
    "global_load_dwordx4 v[152:155], v166, s[40:41] offset:0\n\t" \
    "global_load_dwordx2 v[164:165], v167, s[40:41] offset:0\n\t" \
    "global_load_dwordx4 v[156:159], v168, s[42:43] offset:0\n\t" \
    "global_load_dwordx4 v[160:163], v168, s[42:43] offset:672\n\t" \
    "v_exp_f32 v80, v80\n\t" \
    "v_exp_f32 v81, v81\n\t" \
    "v_exp_f32 v82, v82\n\t" \
    "v_exp_f32 v83, v83\n\t" \
    "v_cvt_pk_bf16_f32 v104, v80, v81\n\t" \
    "v_exp_f32 v84, v84\n\t" \
    "v_mfma_f32_32x32x8_bf16 v[64:79], v[136:137], v[120:121], v[64:79]\n\t" \
    "v_exp_f32 v85, v85\n\t" \
    "v_cvt_pk_bf16_f32 v105, v82, v83\n\t" \
    "v_exp_f32 v86, v86\n\t" \
    "v_exp_f32 v87, v87\n\t" \
    "v_cvt_pk_bf16_f32 v106, v84, v85\n\t" \
    "v_exp_f32 v88, v88\n\t" \
    "v_mfma_f32_32x32x16_bf16 v[0:15], v[128:131], v[96:99], v[0:15]\n\t" \
    "v_exp_f32 v89, v89\n\t" \
    "v_cvt_pk_bf16_f32 v107, v86, v87\n\t" \
    "v_exp_f32 v90, v90\n\t" \
    "v_exp_f32 v91, v91\n\t" \
    "v_cvt_pk_bf16_f32 v108, v88, v89\n\t" \
    "v_exp_f32 v92, v92\n\t" \
    "v_mfma_f32_32x32x16_bf16 v[0:15], v[132:135], v[100:103], v[0:15]\n\t" \
    "v_exp_f32 v93, v93\n\t" \
    "v_cvt_pk_bf16_f32 v109, v90, v91\n\t" \
    "v_exp_f32 v94, v94\n\t" \
    "v_exp_f32 v95, v95\n\t" \
    "v_cvt_pk_bf16_f32 v110, v92, v93\n\t" \
    "v_cvt_pk_bf16_f32 v111, v94, v95\n\t" \
    "v_mfma_f32_32x32x16_bf16 v[80:95], v[124:127], v[116:119], v[48:63]\n\t" \
    "v_exp_f32 v64, v64\n\t" \
    "v_exp_f32 v65, v65\n\t" \
    "v_exp_f32 v66, v66\n\t" \
    "v_exp_f32 v67, v67\n\t" \
    "v_cvt_pk_bf16_f32 v96, v64, v65\n\t" \
    "v_exp_f32 v68, v68\n\t" \
    "v_mfma_f32_32x32x8_bf16 v[80:95], v[136:137], v[122:123], v[80:95]\n\t" \
    "v_exp_f32 v69, v69\n\t" \
    "v_cvt_pk_bf16_f32 v97, v66, v67\n\t" \
    "v_exp_f32 v70, v70\n\t" \
    "v_exp_f32 v71, v71\n\t" \
    "v_cvt_pk_bf16_f32 v98, v68, v69\n\t" \
    "v_exp_f32 v72, v72\n\t" \
    "v_mfma_f32_32x32x16_bf16 v[16:31], v[128:131], v[104:107], v[16:31]\n\t" \
    "v_exp_f32 v73, v73\n\t" \
    "v_cvt_pk_bf16_f32 v99, v70, v71\n\t" \
    "v_exp_f32 v74, v74\n\t" \
    "v_exp_f32 v75, v75\n\t" \
    "v_cvt_pk_bf16_f32 v100, v72, v73\n\t" \
    "v_exp_f32 v76, v76\n\t" \
    "v_mfma_f32_32x32x16_bf16 v[16:31], v[132:135], v[108:111], v[16:31]\n\t" \
    "v_exp_f32 v77, v77\n\t" \
    "v_cvt_pk_bf16_f32 v101, v74, v75\n\t" \
    "v_exp_f32 v78, v78\n\t" \
    "v_exp_f32 v79, v79\n\t" \
    "v_cvt_pk_bf16_f32 v102, v76, v77\n\t" \
    "v_cvt_pk_bf16_f32 v103, v78, v79\n\t" \
    "v_mfma_f32_32x32x16_bf16 v[80:95], v[138:141], v[116:119], v[48:63]\n\t" \
    "v_exp_f32 v64, v64\n\t" \
    "v_exp_f32 v65, v65\n\t" \
    "v_exp_f32 v66, v66\n\t" \
    "v_exp_f32 v67, v67\n\t" \
    "v_cvt_pk_bf16_f32 v96, v64, v65\n\t" \
    "v_exp_f32 v68, v68\n\t" \
    "v_mfma_f32_32x32x8_bf16 v[80:95], v[150:151], v[122:123], v[80:95]\n\t" \
    "v_exp_f32 v69, v69\n\t" \
    "v_cvt_pk_bf16_f32 v97, v66, v67\n\t" \
    "v_exp_f32 v70, v70\n\t" \
    "v_exp_f32 v71, v71\n\t" \
    "v_cvt_pk_bf16_f32 v98, v68, v69\n\t" \
    "v_exp_f32 v72, v72\n\t" \
    "v_mfma_f32_32x32x16_bf16 v[16:31], v[142:145], v[104:107], v[16:31]\n\t" \
    "v_exp_f32 v73, v73\n\t" \
    "v_cvt_pk_bf16_f32 v99, v70, v71\n\t" \
    "v_exp_f32 v74, v74\n\t" \
    "v_exp_f32 v75, v75\n\t" \
    "v_cvt_pk_bf16_f32 v100, v72, v73\n\t" \
    "v_exp_f32 v76, v76\n\t" \
    "v_mfma_f32_32x32x16_bf16 v[16:31], v[146:149], v[108:111], v[16:31]\n\t" \
    "v_exp_f32 v77, v77\n\t" \
    "v_cvt_pk_bf16_f32 v101, v74, v75\n\t" \
    "v_exp_f32 v78, v78\n\t" \
    "v_exp_f32 v79, v79\n\t" \
    "v_cvt_pk_bf16_f32 v102, v76, v77\n\t" \
    "v_cvt_pk_bf16_f32 v103, v78, v79\n\t" \
    "v_mfma_f32_32x32x16_bf16 v[64:79], v[138:141], v[112:115], v[32:47]\n\t" \
    "global_load_dwordx4 v[124:127], v166, s[40:41] offset:1280\n\t" \
    "global_load_dwordx2 v[136:137], v167, s[40:41] offset:1280\n\t" \
    "global_load_dwordx4 v[128:131], v168, s[42:43] offset:1344\n\t" \
    "global_load_dwordx4 v[132:135], v168, s[42:43] offset:2016\n\t" \
    "v_exp_f32 v80, v80\n\t" \
    "v_exp_f32 v81, v81\n\t" \
    "v_exp_f32 v82, v82\n\t" \
    "v_exp_f32 v83, v83\n\t" \
    "v_cvt_pk_bf16_f32 v104, v80, v81\n\t" \
    "v_exp_f32 v84, v84\n\t" \
    "v_mfma_f32_32x32x8_bf16 v[64:79], v[150:151], v[120:121], v[64:79]\n\t" \
    "v_exp_f32 v85, v85\n\t" \
    "v_cvt_pk_bf16_f32 v105, v82, v83\n\t" \
    "v_exp_f32 v86, v86\n\t" \
    "v_exp_f32 v87, v87\n\t" \
    "v_cvt_pk_bf16_f32 v106, v84, v85\n\t" \
    "v_exp_f32 v88, v88\n\t" \
    "v_mfma_f32_32x32x16_bf16 v[0:15], v[142:145], v[96:99], v[0:15]\n\t" \
    "v_exp_f32 v89, v89\n\t" \
    "v_cvt_pk_bf16_f32 v107, v86, v87\n\t" \
    "v_exp_f32 v90, v90\n\t" \
    "v_exp_f32 v91, v91\n\t" \
    "v_cvt_pk_bf16_f32 v108, v88, v89\n\t" \
    "v_exp_f32 v92, v92\n\t" \
    "v_mfma_f32_32x32x16_bf16 v[0:15], v[146:149], v[100:103], v[0:15]\n\t" \
    "v_exp_f32 v93, v93\n\t" \
    "v_cvt_pk_bf16_f32 v109, v90, v91\n\t" \
    "v_exp_f32 v94, v94\n\t" \
    "v_exp_f32 v95, v95\n\t" \
    "v_cvt_pk_bf16_f32 v110, v92, v93\n\t" \
    "v_cvt_pk_bf16_f32 v111, v94, v95\n\t" \
    "v_mfma_f32_32x32x16_bf16 v[80:95], v[138:141], v[116:119], v[48:63]\n\t" \
    "v_exp_f32 v64, v64\n\t" \
    "v_exp_f32 v65, v65\n\t" \
    "v_exp_f32 v66, v66\n\t" \
    "v_exp_f32 v67, v67\n\t" \
    "v_cvt_pk_bf16_f32 v96, v64, v65\n\t" \
    "v_exp_f32 v68, v68\n\t" \
    "v_mfma_f32_32x32x8_bf16 v[80:95], v[150:151], v[122:123], v[80:95]\n\t" \
    "v_exp_f32 v69, v69\n\t" \
    "v_cvt_pk_bf16_f32 v97, v66, v67\n\t" \
    "v_exp_f32 v70, v70\n\t" \
    "v_exp_f32 v71, v71\n\t" \
    "v_cvt_pk_bf16_f32 v98, v68, v69\n\t" \
    "v_exp_f32 v72, v72\n\t" \
    "v_mfma_f32_32x32x16_bf16 v[16:31], v[142:145], v[104:107], v[16:31]\n\t" \
    "v_exp_f32 v73, v73\n\t" \
    "v_cvt_pk_bf16_f32 v99, v70, v71\n\t" \
    "v_exp_f32 v74, v74\n\t" \
    "v_exp_f32 v75, v75\n\t" \
    "v_cvt_pk_bf16_f32 v100, v72, v73\n\t" \
    "v_exp_f32 v76, v76\n\t" \
    "v_mfma_f32_32x32x16_bf16 v[16:31], v[146:149], v[108:111], v[16:31]\n\t" \
    "v_exp_f32 v77, v77\n\t" \
    "v_cvt_pk_bf16_f32 v101, v74, v75\n\t" \
    "v_exp_f32 v78, v78\n\t" \
    "v_exp_f32 v79, v79\n\t" \
    "v_cvt_pk_bf16_f32 v102, v76, v77\n\t" \
    "v_cvt_pk_bf16_f32 v103, v78, v79\n\t" \
    "v_mfma_f32_32x32x16_bf16 v[80:95], v[152:155], v[116:119], v[48:63]\n\t" \
    "v_exp_f32 v64, v64\n\t" \
    "v_exp_f32 v65, v65\n\t" \
    "v_exp_f32 v66, v66\n\t" \
    "v_exp_f32 v67, v67\n\t" \
    "v_cvt_pk_bf16_f32 v96, v64, v65\n\t" \
    "v_exp_f32 v68, v68\n\t" \
    "v_mfma_f32_32x32x8_bf16 v[80:95], v[164:165], v[122:123], v[80:95]\n\t" \
    "v_exp_f32 v69, v69\n\t" \
    "v_cvt_pk_bf16_f32 v97, v66, v67\n\t" \
    "v_exp_f32 v70, v70\n\t" \
    "v_exp_f32 v71, v71\n\t" \
    "v_cvt_pk_bf16_f32 v98, v68, v69\n\t" \
    "v_exp_f32 v72, v72\n\t" \
    "v_mfma_f32_32x32x16_bf16 v[16:31], v[156:159], v[104:107], v[16:31]\n\t" \
    "v_exp_f32 v73, v73\n\t" \
    "v_cvt_pk_bf16_f32 v99, v70, v71\n\t" \
    "v_exp_f32 v74, v74\n\t" \
    "v_exp_f32 v75, v75\n\t" \
    "v_cvt_pk_bf16_f32 v100, v72, v73\n\t" \
    "v_exp_f32 v76, v76\n\t" \
    "v_mfma_f32_32x32x16_bf16 v[16:31], v[160:163], v[108:111], v[16:31]\n\t" \
    "v_exp_f32 v77, v77\n\t" \
    "v_cvt_pk_bf16_f32 v101, v74, v75\n\t" \
    "v_exp_f32 v78, v78\n\t" \
    "v_exp_f32 v79, v79\n\t" \
    "v_cvt_pk_bf16_f32 v102, v76, v77\n\t" \
    "v_cvt_pk_bf16_f32 v103, v78, v79\n\t" \
    "v_mfma_f32_32x32x16_bf16 v[64:79], v[152:155], v[112:115], v[32:47]\n\t" \
    "global_load_dwordx4 v[138:141], v166, s[40:41] offset:2560\n\t" \
    "global_load_dwordx2 v[150:151], v167, s[40:41] offset:2560\n\t" \
    "global_load_dwordx4 v[142:145], v168, s[42:43] offset:2688\n\t" \
    "global_load_dwordx4 v[146:149], v168, s[42:43] offset:3360\n\t" \
    "s_add_u32 s40, s40, 3840\n\t" \
    "s_addc_u32 s41, s41, 0\n\t" \
    "s_add_u32 s42, s42, 4032\n\t" \
    "s_addc_u32 s43, s43, 0\n\t" \
    "s_add_i32 s48, s48, -1\n\t" \
    "v_exp_f32 v80, v80\n\t" \
    "v_exp_f32 v81, v81\n\t" \
    "v_exp_f32 v82, v82\n\t" \
    "v_exp_f32 v83, v83\n\t" \
    "v_cvt_pk_bf16_f32 v104, v80, v81\n\t" \
    "v_exp_f32 v84, v84\n\t" \
    "v_mfma_f32_32x32x8_bf16 v[64:79], v[164:165], v[120:121], v[64:79]\n\t" \
    "v_exp_f32 v85, v85\n\t" \
    "v_cvt_pk_bf16_f32 v105, v82, v83\n\t" \
    "v_exp_f32 v86, v86\n\t" \
    "v_exp_f32 v87, v87\n\t" \
    "v_cvt_pk_bf16_f32 v106, v84, v85\n\t" \
    "v_exp_f32 v88, v88\n\t" \
    "v_mfma_f32_32x32x16_bf16 v[0:15], v[156:159], v[96:99], v[0:15]\n\t" \
    "v_exp_f32 v89, v89\n\t" \
    "v_cvt_pk_bf16_f32 v107, v86, v87\n\t" \
    "v_exp_f32 v90, v90\n\t" \
    "v_exp_f32 v91, v91\n\t" \
    "v_cvt_pk_bf16_f32 v108, v88, v89\n\t" \
    "v_exp_f32 v92, v92\n\t" \
    "v_mfma_f32_32x32x16_bf16 v[0:15], v[160:163], v[100:103], v[0:15]\n\t" \
    "v_exp_f32 v93, v93\n\t" \
    "v_cvt_pk_bf16_f32 v109, v90, v91\n\t" \
    "v_exp_f32 v94, v94\n\t" \
    "v_exp_f32 v95, v95\n\t" \
    "v_cvt_pk_bf16_f32 v110, v92, v93\n\t" \
    "v_cvt_pk_bf16_f32 v111, v94, v95\n\t" \
    "v_mfma_f32_32x32x16_bf16 v[80:95], v[152:155], v[116:119], v[48:63]\n\t" \
    "v_exp_f32 v64, v64\n\t" \
    "v_exp_f32 v65, v65\n\t" \
    "v_exp_f32 v66, v66\n\t" \
    "v_exp_f32 v67, v67\n\t" \
    "v_cvt_pk_bf16_f32 v96, v64, v65\n\t" \
    "v_exp_f32 v68, v68\n\t" \
    "v_mfma_f32_32x32x8_bf16 v[80:95], v[164:165], v[122:123], v[80:95]\n\t" \
    "v_exp_f32 v69, v69\n\t" \
    "v_cvt_pk_bf16_f32 v97, v66, v67\n\t" \
    "v_exp_f32 v70, v70\n\t" \
    "v_exp_f32 v71, v71\n\t" \
    "v_cvt_pk_bf16_f32 v98, v68, v69\n\t" \
    "v_exp_f32 v72, v72\n\t" \
    "v_mfma_f32_32x32x16_bf16 v[16:31], v[156:159], v[104:107], v[16:31]\n\t" \
    "v_exp_f32 v73, v73\n\t" \
    "v_cvt_pk_bf16_f32 v99, v70, v71\n\t" \
    "v_exp_f32 v74, v74\n\t" \
    "v_exp_f32 v75, v75\n\t" \
    "v_cvt_pk_bf16_f32 v100, v72, v73\n\t" \
    "v_exp_f32 v76, v76\n\t" \
    "v_mfma_f32_32x32x16_bf16 v[16:31], v[160:163], v[108:111], v[16:31]\n\t" \
    "v_exp_f32 v77, v77\n\t" \
    "v_cvt_pk_bf16_f32 v101, v74, v75\n\t" \
    "v_exp_f32 v78, v78\n\t" \
    "v_exp_f32 v79, v79\n\t" \
    "v_cvt_pk_bf16_f32 v102, v76, v77\n\t" \
    "v_cvt_pk_bf16_f32 v103, v78, v79\n\t" \
    "s_cmp_lg_u32 s48, 0\n\t" \
    "s_cbranch_scc1 1b\n\t" \
    ""
#define ATTN_ASM_TAIL32 \
    "v_mfma_f32_32x32x16_bf16 v[80:95], v[124:127], v[116:119], v[48:63]\n\t" \
    "v_exp_f32 v64, v64\n\t" \
    "v_exp_f32 v65, v65\n\t" \
    "v_exp_f32 v66, v66\n\t" \
    "v_exp_f32 v67, v67\n\t" \
    "v_cvt_pk_bf16_f32 v96, v64, v65\n\t" \
    "v_exp_f32 v68, v68\n\t" \
    "v_mfma_f32_32x32x8_bf16 v[80:95], v[136:137], v[122:123], v[80:95]\n\t" \
    "v_exp_f32 v69, v69\n\t" \
    "v_cvt_pk_bf16_f32 v97, v66, v67\n\t" \
    "v_exp_f32 v70, v70\n\t" \
    "v_exp_f32 v71, v71\n\t" \
    "v_cvt_pk_bf16_f32 v98, v68, v69\n\t" \
    "v_exp_f32 v72, v72\n\t" \
    "v_mfma_f32_32x32x16_bf16 v[16:31], v[128:131], v[104:107], v[16:31]\n\t" \
    "v_exp_f32 v73, v73\n\t" \
    "v_cvt_pk_bf16_f32 v99, v70, v71\n\t" \
    "v_exp_f32 v74, v74\n\t" \
    "v_exp_f32 v75, v75\n\t" \
    "v_cvt_pk_bf16_f32 v100, v72, v73\n\t" \
    "v_exp_f32 v76, v76\n\t" \
    "v_mfma_f32_32x32x16_bf16 v[16:31], v[132:135], v[108:111], v[16:31]\n\t" \
    "v_exp_f32 v77, v77\n\t" \
    "v_cvt_pk_bf16_f32 v101, v74, v75\n\t" \
    "v_exp_f32 v78, v78\n\t" \
    "v_exp_f32 v79, v79\n\t" \
    "v_cvt_pk_bf16_f32 v102, v76, v77\n\t" \
    "v_cvt_pk_bf16_f32 v103, v78, v79\n\t" \
    "v_mfma_f32_32x32x16_bf16 v[64:79], v[124:127], v[112:115], v[32:47]\n\t" \
    "v_exp_f32 v80, v80\n\t" \
    "v_exp_f32 v81, v81\n\t" \
    "v_exp_f32 v82, v82\n\t" \
    "v_exp_f32 v83, v83\n\t" \
    "v_cvt_pk_bf16_f32 v104, v80, v81\n\t" \
    "v_exp_f32 v84, v84\n\t" \
    "v_mfma_f32_32x32x8_bf16 v[64:79], v[136:137], v[120:121], v[64:79]\n\t" \
    "v_exp_f32 v85, v85\n\t" \
    "v_cvt_pk_bf16_f32 v105, v82, v83\n\t" \
    "v_exp_f32 v86, v86\n\t" \
    "v_exp_f32 v87, v87\n\t" \
    "v_cvt_pk_bf16_f32 v106, v84, v85\n\t" \
    "v_exp_f32 v88, v88\n\t" \
    "v_mfma_f32_32x32x16_bf16 v[0:15], v[128:131], v[96:99], v[0:15]\n\t" \
    "v_exp_f32 v89, v89\n\t" \
    "v_cvt_pk_bf16_f32 v107, v86, v87\n\t" \
    "v_exp_f32 v90, v90\n\t" \
    "v_exp_f32 v91, v91\n\t" \
    "v_cvt_pk_bf16_f32 v108, v88, v89\n\t" \
    "v_exp_f32 v92, v92\n\t" \
    "v_mfma_f32_32x32x16_bf16 v[0:15], v[132:135], v[100:103], v[0:15]\n\t" \
    "v_exp_f32 v93, v93\n\t" \
    "v_cvt_pk_bf16_f32 v109, v90, v91\n\t" \
    "v_exp_f32 v94, v94\n\t" \
    "v_exp_f32 v95, v95\n\t" \
    "v_cvt_pk_bf16_f32 v110, v92, v93\n\t" \
    "v_cvt_pk_bf16_f32 v111, v94, v95\n\t" \
    "v_mfma_f32_32x32x16_bf16 v[80:95], v[124:127], v[116:119], v[48:63]\n\t" \
    "v_exp_f32 v64, v64\n\t" \
    "v_exp_f32 v65, v65\n\t" \
    "v_exp_f32 v66, v66\n\t" \
    "v_exp_f32 v67, v67\n\t" \
    "v_cvt_pk_bf16_f32 v96, v64, v65\n\t" \
    "v_exp_f32 v68, v68\n\t" \
    "v_mfma_f32_32x32x8_bf16 v[80:95], v[136:137], v[122:123], v[80:95]\n\t" \
    "v_exp_f32 v69, v69\n\t" \
    "v_cvt_pk_bf16_f32 v97, v66, v67\n\t" \
    "v_exp_f32 v70, v70\n\t" \
    "v_exp_f32 v71, v71\n\t" \
    "v_cvt_pk_bf16_f32 v98, v68, v69\n\t" \
    "v_exp_f32 v72, v72\n\t" \
    "v_mfma_f32_32x32x16_bf16 v[16:31], v[128:131], v[104:107], v[16:31]\n\t" \
    "v_exp_f32 v73, v73\n\t" \
    "v_cvt_pk_bf16_f32 v99, v70, v71\n\t" \
    "v_exp_f32 v74, v74\n\t" \
    "v_exp_f32 v75, v75\n\t" \
    "v_cvt_pk_bf16_f32 v100, v72, v73\n\t" \
    "v_exp_f32 v76, v76\n\t" \
    "v_mfma_f32_32x32x16_bf16 v[16:31], v[132:135], v[108:111], v[16:31]\n\t" \
    "v_exp_f32 v77, v77\n\t" \
    "v_cvt_pk_bf16_f32 v101, v74, v75\n\t" \
    "v_exp_f32 v78, v78\n\t" \
    "v_exp_f32 v79, v79\n\t" \
    "v_cvt_pk_bf16_f32 v102, v76, v77\n\t" \
    "v_cvt_pk_bf16_f32 v103, v78, v79\n\t" \
    "v_exp_f32 v80, v80\n\t" \
    "v_exp_f32 v81, v81\n\t" \
    "v_exp_f32 v82, v82\n\t" \
    "v_exp_f32 v83, v83\n\t" \
    "v_cvt_pk_bf16_f32 v104, v80, v81\n\t" \
    "v_exp_f32 v84, v84\n\t" \
    "v_exp_f32 v85, v85\n\t" \
    "v_cvt_pk_bf16_f32 v105, v82, v83\n\t" \
    "v_exp_f32 v86, v86\n\t" \
    "v_exp_f32 v87, v87\n\t" \
    "v_cvt_pk_bf16_f32 v106, v84, v85\n\t" \
    "v_exp_f32 v88, v88\n\t" \
    "v_mfma_f32_32x32x16_bf16 v[0:15], v[142:145], v[96:99], v[0:15]\n\t" \
    "v_exp_f32 v89, v89\n\t" \
    "v_cvt_pk_bf16_f32 v107, v86, v87\n\t" \
    "v_exp_f32 v90, v90\n\t" \
    "v_exp_f32 v91, v91\n\t" \
    "v_cvt_pk_bf16_f32 v108, v88, v89\n\t" \
    "v_exp_f32 v92, v92\n\t" \
    "v_mfma_f32_32x32x16_bf16 v[0:15], v[146:149], v[100:103], v[0:15]\n\t" \
    "v_exp_f32 v93, v93\n\t" \
    "v_cvt_pk_bf16_f32 v109, v90, v91\n\t" \
    "v_exp_f32 v94, v94\n\t" \
    "v_exp_f32 v95, v95\n\t" \
    "v_cvt_pk_bf16_f32 v110, v92, v93\n\t" \
    "v_cvt_pk_bf16_f32 v111, v94, v95\n\t" \
    "v_exp_f32 v80, v80\n\t" \
    "v_exp_f32 v81, v81\n\t" \
    "v_exp_f32 v82, v82\n\t" \
    "v_exp_f32 v83, v83\n\t" \
    "v_cvt_pk_bf16_f32 v104, v80, v81\n\t" \
    "v_exp_f32 v84, v84\n\t" \
    "v_exp_f32 v85, v85\n\t" \
    "v_cvt_pk_bf16_f32 v105, v82, v83\n\t" \
    "v_exp_f32 v86, v86\n\t" \
    "v_exp_f32 v87, v87\n\t" \
    "v_cvt_pk_bf16_f32 v106, v84, v85\n\t" \
    "v_exp_f32 v88, v88\n\t" \
    "v_mfma_f32_32x32x16_bf16 v[0:15], v[142:145], v[96:99], v[0:15]\n\t" \
    "v_exp_f32 v89, v89\n\t" \
    "v_cvt_pk_bf16_f32 v107, v86, v87\n\t" \
    "v_exp_f32 v90, v90\n\t" \
    "v_exp_f32 v91, v91\n\t" \
    "v_cvt_pk_bf16_f32 v108, v88, v89\n\t" \
    "v_exp_f32 v92, v92\n\t" \
    "v_mfma_f32_32x32x16_bf16 v[0:15], v[146:149], v[100:103], v[0:15]\n\t" \
    "v_exp_f32 v93, v93\n\t" \
    "v_cvt_pk_bf16_f32 v109, v90, v91\n\t" \
    "v_exp_f32 v94, v94\n\t" \
    "v_exp_f32 v95, v95\n\t" \
    "v_cvt_pk_bf16_f32 v110, v92, v93\n\t" \
    "v_cvt_pk_bf16_f32 v111, v94, v95\n\t" \
    "s_nop 1\n\t" \
    "v_mfma_f32_32x32x16_bf16 v[16:31], v[142:145], v[104:107], v[16:31]\n\t" \
    "v_mfma_f32_32x32x16_bf16 v[16:31], v[146:149], v[108:111], v[16:31]\n\t" \
    "s_nop 15\n\t" \
    ""
#define ATTN_ASM_TAIL64 \
    "v_mfma_f32_32x32x16_bf16 v[80:95], v[124:127], v[116:119], v[48:63]\n\t" \
    "v_exp_f32 v64, v64\n\t" \
    "v_exp_f32 v65, v65\n\t" \
    "v_exp_f32 v66, v66\n\t" \
    "v_exp_f32 v67, v67\n\t" \
    "v_cvt_pk_bf16_f32 v96, v64, v65\n\t" \
    "v_exp_f32 v68, v68\n\t" \
    "v_mfma_f32_32x32x8_bf16 v[80:95], v[136:137], v[122:123], v[80:95]\n\t" \
    "v_exp_f32 v69, v69\n\t" \
    "v_cvt_pk_bf16_f32 v97, v66, v67\n\t" \
    "v_exp_f32 v70, v70\n\t" \
    "v_exp_f32 v71, v71\n\t" \
    "v_cvt_pk_bf16_f32 v98, v68, v69\n\t" \
    "v_exp_f32 v72, v72\n\t" \
    "v_mfma_f32_32x32x16_bf16 v[16:31], v[128:131], v[104:107], v[16:31]\n\t" \
    "v_exp_f32 v73, v73\n\t" \
    "v_cvt_pk_bf16_f32 v99, v70, v71\n\t" \
    "v_exp_f32 v74, v74\n\t" \
    "v_exp_f32 v75, v75\n\t" \
    "v_cvt_pk_bf16_f32 v100, v72, v73\n\t" \
    "v_exp_f32 v76, v76\n\t" \
    "v_mfma_f32_32x32x16_bf16 v[16:31], v[132:135], v[108:111], v[16:31]\n\t" \
    "v_exp_f32 v77, v77\n\t" \
    "v_cvt_pk_bf16_f32 v101, v74, v75\n\t" \
    "v_exp_f32 v78, v78\n\t" \
    "v_exp_f32 v79, v79\n\t" \
    "v_cvt_pk_bf16_f32 v102, v76, v77\n\t" \
    "v_cvt_pk_bf16_f32 v103, v78, v79\n\t" \
    "v_mfma_f32_32x32x16_bf16 v[64:79], v[124:127], v[112:115], v[32:47]\n\t" \
    "global_load_dwordx4 v[152:155], v166, s[40:41] offset:0\n\t" \
    "global_load_dwordx2 v[164:165], v167, s[40:41] offset:0\n\t" \
    "global_load_dwordx4 v[156:159], v168, s[42:43] offset:0\n\t" \
    "global_load_dwordx4 v[160:163], v168, s[42:43] offset:672\n\t" \
    "v_exp_f32 v80, v80\n\t" \
    "v_exp_f32 v81, v81\n\t" \
    "v_exp_f32 v82, v82\n\t" \
    "v_exp_f32 v83, v83\n\t" \
    "v_cvt_pk_bf16_f32 v104, v80, v81\n\t" \
    "v_exp_f32 v84, v84\n\t" \
    "v_mfma_f32_32x32x8_bf16 v[64:79], v[136:137], v[120:121], v[64:79]\n\t" \
    "v_exp_f32 v85, v85\n\t" \
    "v_cvt_pk_bf16_f32 v105, v82, v83\n\t" \
    "v_exp_f32 v86, v86\n\t" \
    "v_exp_f32 v87, v87\n\t" \
    "v_cvt_pk_bf16_f32 v106, v84, v85\n\t" \
    "v_exp_f32 v88, v88\n\t" \
    "v_mfma_f32_32x32x16_bf16 v[0:15], v[128:131], v[96:99], v[0:15]\n\t" \
    "v_exp_f32 v89, v89\n\t" \
    "v_cvt_pk_bf16_f32 v107, v86, v87\n\t" \
    "v_exp_f32 v90, v90\n\t" \
    "v_exp_f32 v91, v91\n\t" \
    "v_cvt_pk_bf16_f32 v108, v88, v89\n\t" \
    "v_exp_f32 v92, v92\n\t" \
    "v_mfma_f32_32x32x16_bf16 v[0:15], v[132:135], v[100:103], v[0:15]\n\t" \
    "v_exp_f32 v93, v93\n\t" \
    "v_cvt_pk_bf16_f32 v109, v90, v91\n\t" \
    "v_exp_f32 v94, v94\n\t" \
    "v_exp_f32 v95, v95\n\t" \
    "v_cvt_pk_bf16_f32 v110, v92, v93\n\t" \
    "v_cvt_pk_bf16_f32 v111, v94, v95\n\t" \
    "v_mfma_f32_32x32x16_bf16 v[80:95], v[124:127], v[116:119], v[48:63]\n\t" \
    "v_exp_f32 v64, v64\n\t" \
    "v_exp_f32 v65, v65\n\t" \
    "v_exp_f32 v66, v66\n\t" \
    "v_exp_f32 v67, v67\n\t" \
    "v_cvt_pk_bf16_f32 v96, v64, v65\n\t" \
    "v_exp_f32 v68, v68\n\t" \
    "v_mfma_f32_32x32x8_bf16 v[80:95], v[136:137], v[122:123], v[80:95]\n\t" \
    "v_exp_f32 v69, v69\n\t" \
    "v_cvt_pk_bf16_f32 v97, v66, v67\n\t" \
    "v_exp_f32 v70, v70\n\t" \
    "v_exp_f32 v71, v71\n\t" \
    "v_cvt_pk_bf16_f32 v98, v68, v69\n\t" \
    "v_exp_f32 v72, v72\n\t" \
    "v_mfma_f32_32x32x16_bf16 v[16:31], v[128:131], v[104:107], v[16:31]\n\t" \
    "v_exp_f32 v73, v73\n\t" \
    "v_cvt_pk_bf16_f32 v99, v70, v71\n\t" \
    "v_exp_f32 v74, v74\n\t" \
    "v_exp_f32 v75, v75\n\t" \
    "v_cvt_pk_bf16_f32 v100, v72, v73\n\t" \
    "v_exp_f32 v76, v76\n\t" \
    "v_mfma_f32_32x32x16_bf16 v[16:31], v[132:135], v[108:111], v[16:31]\n\t" \
    "v_exp_f32 v77, v77\n\t" \
    "v_cvt_pk_bf16_f32 v101, v74, v75\n\t" \
    "v_exp_f32 v78, v78\n\t" \
    "v_exp_f32 v79, v79\n\t" \
    "v_cvt_pk_bf16_f32 v102, v76, v77\n\t" \
    "v_cvt_pk_bf16_f32 v103, v78, v79\n\t" \
    "v_mfma_f32_32x32x16_bf16 v[80:95], v[138:141], v[116:119], v[48:63]\n\t" \
    "v_exp_f32 v64, v64\n\t" \
    "v_exp_f32 v65, v65\n\t" \
    "v_exp_f32 v66, v66\n\t" \
    "v_exp_f32 v67, v67\n\t" \
    "v_cvt_pk_bf16_f32 v96, v64, v65\n\t" \
    "v_exp_f32 v68, v68\n\t" \
    "v_mfma_f32_32x32x8_bf16 v[80:95], v[150:151], v[122:123], v[80:95]\n\t" \
    "v_exp_f32 v69, v69\n\t" \
    "v_cvt_pk_bf16_f32 v97, v66, v67\n\t" \
    "v_exp_f32 v70, v70\n\t" \
    "v_exp_f32 v71, v71\n\t" \
    "v_cvt_pk_bf16_f32 v98, v68, v69\n\t" \
    "v_exp_f32 v72, v72\n\t" \
    "v_mfma_f32_32x32x16_bf16 v[16:31], v[142:145], v[104:107], v[16:31]\n\t" \
    "v_exp_f32 v73, v73\n\t" \
    "v_cvt_pk_bf16_f32 v99, v70, v71\n\t" \
    "v_exp_f32 v74, v74\n\t" \
    "v_exp_f32 v75, v75\n\t" \
    "v_cvt_pk_bf16_f32 v100, v72, v73\n\t" \
    "v_exp_f32 v76, v76\n\t" \
    "v_mfma_f32_32x32x16_bf16 v[16:31], v[146:149], v[108:111], v[16:31]\n\t" \
    "v_exp_f32 v77, v77\n\t" \
    "v_cvt_pk_bf16_f32 v101, v74, v75\n\t" \
    "v_exp_f32 v78, v78\n\t" \
    "v_exp_f32 v79, v79\n\t" \
    "v_cvt_pk_bf16_f32 v102, v76, v77\n\t" \
    "v_cvt_pk_bf16_f32 v103, v78, v79\n\t" \
    "v_mfma_f32_32x32x16_bf16 v[64:79], v[138:141], v[112:115], v[32:47]\n\t" \
    "global_load_dwordx4 v[124:127], v166, s[40:41] offset:1280\n\t" \
    "global_load_dwordx2 v[136:137], v167, s[40:41] offset:1280\n\t" \
    "global_load_dwordx4 v[128:131], v168, s[42:43] offset:1344\n\t" \
    "global_load_dwordx4 v[132:135], v168, s[42:43] offset:2016\n\t" \
    "v_exp_f32 v80, v80\n\t" \
    "v_exp_f32 v81, v81\n\t" \
    "v_exp_f32 v82, v82\n\t" \
    "v_exp_f32 v83, v83\n\t" \
    "v_cvt_pk_bf16_f32 v104, v80, v81\n\t" \
    "v_exp_f32 v84, v84\n\t" \
    "v_mfma_f32_32x32x8_bf16 v[64:79], v[150:151], v[120:121], v[64:79]\n\t" \
    "v_exp_f32 v85, v85\n\t" \
    "v_cvt_pk_bf16_f32 v105, v82, v83\n\t" \
    "v_exp_f32 v86, v86\n\t" \
    "v_exp_f32 v87, v87\n\t" \
    "v_cvt_pk_bf16_f32 v106, v84, v85\n\t" \
    "v_exp_f32 v88, v88\n\t" \
    "v_mfma_f32_32x32x16_bf16 v[0:15], v[142:145], v[96:99], v[0:15]\n\t" \
    "v_exp_f32 v89, v89\n\t" \
    "v_cvt_pk_bf16_f32 v107, v86, v87\n\t" \
    "v_exp_f32 v90, v90\n\t" \
    "v_exp_f32 v91, v91\n\t" \
    "v_cvt_pk_bf16_f32 v108, v88, v89\n\t" \
    "v_exp_f32 v92, v92\n\t" \
    "v_mfma_f32_32x32x16_bf16 v[0:15], v[146:149], v[100:103], v[0:15]\n\t" \
    "v_exp_f32 v93, v93\n\t" \
    "v_cvt_pk_bf16_f32 v109, v90, v91\n\t" \
    "v_exp_f32 v94, v94\n\t" \
    "v_exp_f32 v95, v95\n\t" \
    "v_cvt_pk_bf16_f32 v110, v92, v93\n\t" \
    "v_cvt_pk_bf16_f32 v111, v94, v95\n\t" \
    "v_mfma_f32_32x32x16_bf16 v[80:95], v[138:141], v[116:119], v[48:63]\n\t" \
    "v_exp_f32 v64, v64\n\t" \
    "v_exp_f32 v65, v65\n\t" \
    "v_exp_f32 v66, v66\n\t" \
    "v_exp_f32 v67, v67\n\t" \
    "v_cvt_pk_bf16_f32 v96, v64, v65\n\t" \
    "v_exp_f32 v68, v68\n\t" \
    "v_mfma_f32_32x32x8_bf16 v[80:95], v[150:151], v[122:123], v[80:95]\n\t" \
    "v_exp_f32 v69, v69\n\t" \
    "v_cvt_pk_bf16_f32 v97, v66, v67\n\t" \
    "v_exp_f32 v70, v70\n\t" \
    "v_exp_f32 v71, v71\n\t" \
    "v_cvt_pk_bf16_f32 v98, v68, v69\n\t" \
    "v_exp_f32 v72, v72\n\t" \
    "v_mfma_f32_32x32x16_bf16 v[16:31], v[142:145], v[104:107], v[16:31]\n\t" \
    "v_exp_f32 v73, v73\n\t" \
    "v_cvt_pk_bf16_f32 v99, v70, v71\n\t" \
    "v_exp_f32 v74, v74\n\t" \
    "v_exp_f32 v75, v75\n\t" \
    "v_cvt_pk_bf16_f32 v100, v72, v73\n\t" \
    "v_exp_f32 v76, v76\n\t" \
    "v_mfma_f32_32x32x16_bf16 v[16:31], v[146:149], v[108:111], v[16:31]\n\t" \
    "v_exp_f32 v77, v77\n\t" \
    "v_cvt_pk_bf16_f32 v101, v74, v75\n\t" \
    "v_exp_f32 v78, v78\n\t" \
    "v_exp_f32 v79, v79\n\t" \
    "v_cvt_pk_bf16_f32 v102, v76, v77\n\t" \
    "v_cvt_pk_bf16_f32 v103, v78, v79\n\t" \
    "v_mfma_f32_32x32x16_bf16 v[80:95], v[152:155], v[116:119], v[48:63]\n\t" \
    "v_exp_f32 v64, v64\n\t" \
    "v_exp_f32 v65, v65\n\t" \
    "v_exp_f32 v66, v66\n\t" \
    "v_exp_f32 v67, v67\n\t" \
    "v_cvt_pk_bf16_f32 v96, v64, v65\n\t" \
    "v_exp_f32 v68, v68\n\t" \
    "v_mfma_f32_32x32x8_bf16 v[80:95], v[164:165], v[122:123], v[80:95]\n\t" \
    "v_exp_f32 v69, v69\n\t" \
    "v_cvt_pk_bf16_f32 v97, v66, v67\n\t" \
    "v_exp_f32 v70, v70\n\t" \
    "v_exp_f32 v71, v71\n\t" \
    "v_cvt_pk_bf16_f32 v98, v68, v69\n\t" \
    "v_exp_f32 v72, v72\n\t" \
    "v_mfma_f32_32x32x16_bf16 v[16:31], v[156:159], v[104:107], v[16:31]\n\t" \
    "v_exp_f32 v73, v73\n\t" \
    "v_cvt_pk_bf16_f32 v99, v70, v71\n\t" \
    "v_exp_f32 v74, v74\n\t" \
    "v_exp_f32 v75, v75\n\t" \
    "v_cvt_pk_bf16_f32 v100, v72, v73\n\t" \
    "v_exp_f32 v76, v76\n\t" \
    "v_mfma_f32_32x32x16_bf16 v[16:31], v[160:163], v[108:111], v[16:31]\n\t" \
    "v_exp_f32 v77, v77\n\t" \
    "v_cvt_pk_bf16_f32 v101, v74, v75\n\t" \
    "v_exp_f32 v78, v78\n\t" \
    "v_exp_f32 v79, v79\n\t" \
    "v_cvt_pk_bf16_f32 v102, v76, v77\n\t" \
    "v_cvt_pk_bf16_f32 v103, v78, v79\n\t" \
    "v_mfma_f32_32x32x16_bf16 v[64:79], v[152:155], v[112:115], v[32:47]\n\t" \
    "v_exp_f32 v80, v80\n\t" \
    "v_exp_f32 v81, v81\n\t" \
    "v_exp_f32 v82, v82\n\t" \
    "v_exp_f32 v83, v83\n\t" \
    "v_cvt_pk_bf16_f32 v104, v80, v81\n\t" \
    "v_exp_f32 v84, v84\n\t" \
    "v_mfma_f32_32x32x8_bf16 v[64:79], v[164:165], v[120:121], v[64:79]\n\t" \
    "v_exp_f32 v85, v85\n\t" \
    "v_cvt_pk_bf16_f32 v105, v82, v83\n\t" \
    "v_exp_f32 v86, v86\n\t" \
    "v_exp_f32 v87, v87\n\t" \
    "v_cvt_pk_bf16_f32 v106, v84, v85\n\t" \
    "v_exp_f32 v88, v88\n\t" \
    "v_mfma_f32_32x32x16_bf16 v[0:15], v[156:159], v[96:99], v[0:15]\n\t" \
    "v_exp_f32 v89, v89\n\t" \
    "v_cvt_pk_bf16_f32 v107, v86, v87\n\t" \
    "v_exp_f32 v90, v90\n\t" \
    "v_exp_f32 v91, v91\n\t" \
    "v_cvt_pk_bf16_f32 v108, v88, v89\n\t" \
    "v_exp_f32 v92, v92\n\t" \
    "v_mfma_f32_32x32x16_bf16 v[0:15], v[160:163], v[100:103], v[0:15]\n\t" \
    "v_exp_f32 v93, v93\n\t" \
    "v_cvt_pk_bf16_f32 v109, v90, v91\n\t" \
    "v_exp_f32 v94, v94\n\t" \
    "v_exp_f32 v95, v95\n\t" \
    "v_cvt_pk_bf16_f32 v110, v92, v93\n\t" \
    "v_cvt_pk_bf16_f32 v111, v94, v95\n\t" \
    "v_mfma_f32_32x32x16_bf16 v[80:95], v[152:155], v[116:119], v[48:63]\n\t" \
    "v_exp_f32 v64, v64\n\t" \
    "v_exp_f32 v65, v65\n\t" \
    "v_exp_f32 v66, v66\n\t" \
    "v_exp_f32 v67, v67\n\t" \
    "v_cvt_pk_bf16_f32 v96, v64, v65\n\t" \
    "v_exp_f32 v68, v68\n\t" \
    "v_mfma_f32_32x32x8_bf16 v[80:95], v[164:165], v[122:123], v[80:95]\n\t" \
    "v_exp_f32 v69, v69\n\t" \
    "v_cvt_pk_bf16_f32 v97, v66, v67\n\t" \
    "v_exp_f32 v70, v70\n\t" \
    "v_exp_f32 v71, v71\n\t" \
    "v_cvt_pk_bf16_f32 v98, v68, v69\n\t" \
    "v_exp_f32 v72, v72\n\t" \
    "v_mfma_f32_32x32x16_bf16 v[16:31], v[156:159], v[104:107], v[16:31]\n\t" \
    "v_exp_f32 v73, v73\n\t" \
    "v_cvt_pk_bf16_f32 v99, v70, v71\n\t" \
    "v_exp_f32 v74, v74\n\t" \
    "v_exp_f32 v75, v75\n\t" \
    "v_cvt_pk_bf16_f32 v100, v72, v73\n\t" \
    "v_exp_f32 v76, v76\n\t" \
    "v_mfma_f32_32x32x16_bf16 v[16:31], v[160:163], v[108:111], v[16:31]\n\t" \
    "v_exp_f32 v77, v77\n\t" \
    "v_cvt_pk_bf16_f32 v101, v74, v75\n\t" \
    "v_exp_f32 v78, v78\n\t" \
    "v_exp_f32 v79, v79\n\t" \
    "v_cvt_pk_bf16_f32 v102, v76, v77\n\t" \
    "v_cvt_pk_bf16_f32 v103, v78, v79\n\t" \
    "v_exp_f32 v80, v80\n\t" \
    "v_exp_f32 v81, v81\n\t" \
    "v_exp_f32 v82, v82\n\t" \
    "v_exp_f32 v83, v83\n\t" \
    "v_cvt_pk_bf16_f32 v104, v80, v81\n\t" \
    "v_exp_f32 v84, v84\n\t" \
    "v_exp_f32 v85, v85\n\t" \
    "v_cvt_pk_bf16_f32 v105, v82, v83\n\t" \
    "v_exp_f32 v86, v86\n\t" \
    "v_exp_f32 v87, v87\n\t" \
    "v_cvt_pk_bf16_f32 v106, v84, v85\n\t" \
    "v_exp_f32 v88, v88\n\t" \
    "v_mfma_f32_32x32x16_bf16 v[0:15], v[128:131], v[96:99], v[0:15]\n\t" \
    "v_exp_f32 v89, v89\n\t" \
    "v_cvt_pk_bf16_f32 v107, v86, v87\n\t" \
    "v_exp_f32 v90, v90\n\t" \
    "v_exp_f32 v91, v91\n\t" \
    "v_cvt_pk_bf16_f32 v108, v88, v89\n\t" \
    "v_exp_f32 v92, v92\n\t" \
    "v_mfma_f32_32x32x16_bf16 v[0:15], v[132:135], v[100:103], v[0:15]\n\t" \
    "v_exp_f32 v93, v93\n\t" \
    "v_cvt_pk_bf16_f32 v109, v90, v91\n\t" \
    "v_exp_f32 v94, v94\n\t" \
    "v_exp_f32 v95, v95\n\t" \
    "v_cvt_pk_bf16_f32 v110, v92, v93\n\t" \
    "v_cvt_pk_bf16_f32 v111, v94, v95\n\t" \
    "v_exp_f32 v80, v80\n\t" \
    "v_exp_f32 v81, v81\n\t" \
    "v_exp_f32 v82, v82\n\t" \
    "v_exp_f32 v83, v83\n\t" \
    "v_cvt_pk_bf16_f32 v104, v80, v81\n\t" \
    "v_exp_f32 v84, v84\n\t" \
    "v_exp_f32 v85, v85\n\t" \
    "v_cvt_pk_bf16_f32 v105, v82, v83\n\t" \
    "v_exp_f32 v86, v86\n\t" \
    "v_exp_f32 v87, v87\n\t" \
    "v_cvt_pk_bf16_f32 v106, v84, v85\n\t" \
    "v_exp_f32 v88, v88\n\t" \
    "v_mfma_f32_32x32x16_bf16 v[0:15], v[128:131], v[96:99], v[0:15]\n\t" \
    "v_exp_f32 v89, v89\n\t" \
    "v_cvt_pk_bf16_f32 v107, v86, v87\n\t" \
    "v_exp_f32 v90, v90\n\t" \
    "v_exp_f32 v91, v91\n\t" \
    "v_cvt_pk_bf16_f32 v108, v88, v89\n\t" \
    "v_exp_f32 v92, v92\n\t" \
    "v_mfma_f32_32x32x16_bf16 v[0:15], v[132:135], v[100:103], v[0:15]\n\t" \
    "v_exp_f32 v93, v93\n\t" \
    "v_cvt_pk_bf16_f32 v109, v90, v91\n\t" \
    "v_exp_f32 v94, v94\n\t" \
    "v_exp_f32 v95, v95\n\t" \
    "v_cvt_pk_bf16_f32 v110, v92, v93\n\t" \
    "v_cvt_pk_bf16_f32 v111, v94, v95\n\t" \
    "s_nop 1\n\t" \
    "v_mfma_f32_32x32x16_bf16 v[16:31], v[128:131], v[104:107], v[16:31]\n\t" \
    "v_mfma_f32_32x32x16_bf16 v[16:31], v[132:135], v[108:111], v[16:31]\n\t" \
    "s_nop 15\n\t" \
    ""
#define VCLOB8(a) "v" #a "0", "v" #a "1", "v" #a "2", "v" #a "3", "v" #a "4", "v" #a "5", "v" #a "6", "v" #a "7", "v" #a "8", "v" #a "9"
#define ATTN_CLOBBERS "v32", "v33", "v34", "v35", "v36", "v37", "v38", "v39", VCLOB8(4), VCLOB8(5), VCLOB8(6), VCLOB8(7), VCLOB8(8), VCLOB8(9), VCLOB8(10), VCLOB8(11), VCLOB8(12), VCLOB8(13), VCLOB8(14), VCLOB8(15), "v160", "v161", "v162", "v163", "v164", "v165", "scc", "memory"
template <int NW>
__global__ __launch_bounds__(NW * 64, NW / 4) void attn_kernel(char* ws) {
    __shared__ float xch[NW == 8 ? 8 : 1][13][64];
    TS_BEGIN(1)
    const int tid = threadIdx.x, lane = tid & 63, wid = __builtin_amdgcn_readfirstlane(tid >> 6), r32 = lane & 31, hf = lane >> 5;
    const int head = (blockIdx.x & 7) * 4 + (blockIdx.x >> 6), qp = (blockIdx.x >> 3) & 7;
    const int qg = wid & 3, kh = wid >> 2, TA = qp * 8 + qg * 2;
    const char* qb = ws + WS_Q + (size_t)(head * 64 + TA) * QK_TILE;
    const char* kb = ws + WS_K + (size_t)head * 64 * QK_TILE;
    const char* vb = ws + WS_V + (size_t)head * 64 * V_TILE;
    const int ko0 = lane * 16, ko1 = 1024 + r32 * 8, vo = hf * 336 + min(r32, DH) * 16;
    f32x16 oA, oB;
    {
        const char* kp = kb + (kh * 32 + 1) * QK_TILE; const char* vp = vb + kh * 32 * V_TILE;
        int cnt = NW == 8 ? 10 : 20;
        if (NW == 8)
            asm volatile(ATTN_ASM_MAIN ATTN_ASM_TAIL32 : "={v[0:15]}"(oA), "={v[16:31]}"(oB), "+{s[40:41]}"(kp), "+{s[42:43]}"(vp), "+{s48}"(cnt)
                         : "{s[44:45]}"(qb), "{s[46:47]}"(kb), "{v166}"(ko0), "{v167}"(ko1), "{v168}"(vo) : ATTN_CLOBBERS);
        else
            asm volatile(ATTN_ASM_MAIN ATTN_ASM_TAIL64 : "={v[0:15]}"(oA), "={v[16:31]}"(oB), "+{s[40:41]}"(kp), "+{s[42:43]}"(vp), "+{s48}"(cnt)
                         : "{s[44:45]}"(qb), "{s[46:47]}"(kb), "{v166}"(ko0), "{v167}"(ko1), "{v168}"(vo) : ATTN_CLOBBERS);
    }
    auto slow = [&](int T) {
        const char* q = ws + WS_Q + (size_t)(head * 64 + T) * QK_TILE;
        const u32x4 q0 = *(const u32x4*)(q + ko0); u32x2 q1 = *(const u32x2*)(q + ko1);
        if (hf) q1 = (u32x2){0u, 0u};
        f32x16 oo = {}; float m = -INFINITY; u32x4 p0, p1;
        for (int t = 0; t < 64; ++t) {
            KV c; kv_load(c, kb + t * QK_TILE, vb + t * V_TILE, ko0, ko1, vo);
            const f32x16 z = {};
            f32x16 s = mfma32k8(c.k1, q1, mfma32(c.k0, q0, z));
            const float mn = fmaxf(m, max16(s)), al = __builtin_amdgcn_exp2f(m - mn);
            m = mn;
#pragma unroll
            for (int i = 0; i < 16; ++i) { s[i] -= mn; oo[i] *= al; }
            exp_pack(s, p0, p1);
            oo = mfma32(c.v0, p0, oo); oo = mfma32(c.v1, p1, oo);
        }
        float o[12];
#pragma unroll
        for (int i = 0; i < 12; ++i) o[i] = oo[i];
        store_o(ws, T, head, lane, o, 1.f / (hf ? oo[8] : oo[12]));
    };
    if (NW == 8) {
#pragma unroll
        for (int i = 0; i < 13; ++i) xch[wid][i][lane] = kh ? oA[i] : oB[i];
        __syncthreads();
        float o[13]; bool bad = false;
#pragma unroll
        for (int i = 0; i < 13; ++i) { o[i] = (kh ? oB[i] : oA[i]) + xch[wid ^ 4][i][lane]; bad |= !(fabsf(o[i]) < 3.0e38f); }
        const float l = hf ? o[8] : o[12];
        bad |= !(l > 0.25f);
        if (!__any(bad)) store_o(ws, TA + kh, head, lane, o, __builtin_amdgcn_rcpf(l)); else slow(TA + kh);
    } else {
#pragma unroll
        for (int b = 0; b < 2; ++b) {
            float o[13]; bool bad = false;
#pragma unroll
            for (int i = 0; i < 13; ++i) { o[i] = b ? oB[i] : oA[i]; bad |= !(fabsf(o[i]) < 3.0e38f); }
            const float l = hf ? o[8] : o[12];
            bad |= !(l > 0.25f);
            if (!__any(bad)) store_o(ws, TA + b, head, lane, o, __builtin_amdgcn_rcpf(l)); else slow(TA + b);
        }
    }
    TS_END(1, ws);
}

template <bool DOWN>
__global__ __launch_bounds__(256, 1) void proj_kernel(const u16* __restrict__ w, const u16* __restrict__ x, char* ws, u16* __restrict__ out) {
    extern __shared__ __attribute__((aligned(16))) char smem[];
    TS_BEGIN(DOWN ? 4 : 2)
    constexpr int KS = DOWN ? 108 : 40, NCH = DOWN ? 14 : 5, NSLOT = DOWN ? 7 : 5, SLOTB = 20480, KROWS = KS * 16;
    const int tid = threadIdx.x, lane = tid & 63, wid = __builtin_amdgcn_readfirstlane(tid >> 6), g = lane >> 4, c16 = lane & 15;
    const int xcd = blockIdx.x & 7, idx = blockIdx.x >> 3;
    const int pm = (xcd >> 1) * 8 + (idx & 7), ct = (xcd & 1) * 4 + (idx >> 3);
    const int T = pm * 2 + (wid >> 1), sub = wid & 1;
    const unsigned lds0 = (unsigned)(uintptr_t)(LAS char*)smem;
    const char* ab = ws + (DOWN ? WS_G : WS_O) + (size_t)T * KS * 1024;
    const unsigned ao = (g >> 1) * 1024 + (g & 1) * 512 + (sub * 16 + c16) * 16;
    unsigned wo_[5];
#pragma unroll
    for (int i = 0; i < 5; ++i) { const int id = (wid * 5 + i) * 64 + lane, row = id / 10, cc = id - row * 10; wo_[i] = (unsigned)(row * (D * 2) + cc * 16 + 4096 - (i & 3) * 1024); }
    unsigned wl_[3];
#pragma unroll
    for (int i = 0; i < 3; ++i) { const int id = min((wid * 3 + i) * 64 + lane, 639), row = id / 10, cc = id - row * 10; wl_[i] = (unsigned)(row * (D * 2) + cc * 16 + 4096 - i * 1024); }
    const char* wb = (const char*)w + ct * 160 - 4096;
    u32x4 af[NSLOT][4];
    auto issue = [&](int c) {
        const char* wsrc = wb + (size_t)c * 128 * (D * 2);
        if (DOWN && c == NCH - 1) {
            const unsigned slot = lds0 + (c % NSLOT) * SLOTB + wid * 3072;
            glds<0>(wsrc, wl_[0], slot); glds<1024>(wsrc, wl_[1], slot); glds<2048>(wsrc, wl_[2], slot);
            gload16<0>(af[c % NSLOT][0], ab + c * 8192, ao); gload16<2048>(af[c % NSLOT][1], ab + c * 8192, ao);
        } else {
            const unsigned slot = lds0 + (c % NSLOT) * SLOTB + wid * 5120;
            glds4(wsrc, wo_[0], wo_[1], wo_[2], wo_[3], slot); glds<0>(wsrc, wo_[4], slot + 4096);
            if (DOWN) {
                gload16<0>(af[c % NSLOT][0], ab + c * 8192, ao); gload16<2048>(af[c % NSLOT][1], ab + c * 8192, ao);
                gload16<0>(af[c % NSLOT][2], ab + c * 8192 + 4096, ao); gload16<2048>(af[c % NSLOT][3], ab + c * 8192 + 4096, ao);
            }
        }
    };
    f32x4 acc[5] = {};
    LAS const char* sm = (LAS const char*)smem;
    const int q4 = (lane >> 2) & 3, p4 = lane & 3;
    const size_t tok = (size_t)T * 32 + sub * 16 + c16;
    char* hp = ws + WS_H + ((size_t)T * 40 + ct * 5) * 1024 + (g >> 1) * 512 + (sub * 16 + c16) * 16 + (g & 1) * 8;
    if constexpr (DOWN) {
#pragma unroll
        for (int c = 0; c < NSLOT; ++c) issue(c);
        const int wl = (16 * (g >> 1) + 4 * (g & 1) + q4) * 160 + p4 * 8;
        u32x4 fw[2][5];
        auto rd = [&](int st, int n5) {
            const int c = st >> 2, m = st & 3, so = (c % NSLOT) * SLOTB, hi = (so / (3 * SLOTB)) * (3 * SLOTB);
            LAS const char* p = sm + (so - hi) + m * 32 * 160 + n5 * 32 + OPQ(wl, hi);
            fw[st & 1][n5] = trfrag(p, p + 8 * 160);
        };
        wait_vm_bar<54>();
#pragma unroll
        for (int m = 0; m < 4; ++m) PIN(af[0][m]);
#pragma unroll
        for (int n5 = 0; n5 < 5; ++n5) rd(0, n5);
        SBAR();
        sfor<54>([&](auto ST) {
            constexpr int st = ST.v, c = st >> 2, m = st & 3;
            if constexpr (m == 3) {
                constexpr int lastc = (c == 0) ? NSLOT - 1 : ((c + NSLOT - 2 < NCH - 1) ? c + NSLOT - 2 : NCH - 1);
                constexpr int n = (lastc > c + 1) ? ((lastc == NCH - 1) ? (lastc - c - 2) * 9 + 5 : (lastc - c - 1) * 9) : 0;
                wait_vm_bar_nl<n>();
#pragma unroll
                for (int mm = 0; mm < ((c + 1 == NCH - 1) ? 2 : 4); ++mm) PIN(af[(c + 1) % NSLOT][mm]);
                if (c >= 1 && c + NSLOT - 1 < NCH) issue(c + NSLOT - 1);
            }
#pragma unroll
            for (int n5 = 0; n5 < 5; ++n5) {
                const u32x4 wf = fw[st & 1][n5];
                if (st + 1 < 54) rd(st + 1, n5);
                acc[n5] = mfma16(wf, af[c % NSLOT][m], acc[n5]);
                SBAR();
            }
        });
#pragma unroll
        for (int n5 = 0; n5 < 5; ++n5) {
            const u32x2 hv = *(const u32x2*)(hp + n5 * 1024);
            *(u32x2*)(out + tok * D + ct * 80 + n5 * 16 + g * 4) = (u32x2){pk2(acc[n5][0] + bf_lo(hv[0]), acc[n5][1] + bf_hi(hv[0])), pk2(acc[n5][2] + bf_lo(hv[1]), acc[n5][3] + bf_hi(hv[1]))};
        }
    } else {
#pragma unroll
        for (int c = 0; c < 5; ++c) {
            issue(c);
            gload16<0>(af[c][0], ab + c * 8192, ao); gload16<2048>(af[c][1], ab + c * 8192, ao);
            gload16<0>(af[c][2], ab + c * 8192 + 4096, ao); gload16<2048>(af[c][3], ab + c * 8192 + 4096, ao);
        }
        const int wl = (20 * (g >> 1) + 4 * (g & 1) + q4) * 160 + p4 * 8;
        const int wt = (80 * (g >> 1) + 20 * (g & 1) + 16 + q4) * 160 + p4 * 8;
        u32x4 fw[2][5];
        auto rd = [&](int st, int n5) {
            if (st < 16) { const int o = st * 40 * 160, hi = o >= 51200 ? 51200 : 0; LAS const char* p = sm + (o - hi) + n5 * 32 + OPQ(wl, hi); fw[st & 1][n5] = trfrag(p, p + 8 * 160); }
            else { const int o = (st - 16) * 160 * 160, hi = o >= 51200 ? 51200 : 0; LAS const char* p = sm + (o - hi) + n5 * 32 + OPQ(wt, hi); fw[st & 1][n5] = trfrag(p, p + 40 * 160); }
        };
        wait_vm_bar<27>();
#pragma unroll
        for (int m = 0; m < 4; ++m) { PIN(af[0][m]); PIN(af[1][m]); }
#pragma unroll
        for (int n5 = 0; n5 < 5; ++n5) rd(0, n5);
        SBAR();
#pragma unroll
        for (int st = 0; st < 20; ++st) {
            const int c = st >> 2, m = st & 3;
            if (m == 3 && c < 3) {
                if (c == 0) wait_vm_bar_nl<18>(); else if (c == 1) wait_vm_bar_nl<9>(); else wait_vm_bar_nl<0>();
#pragma unroll
                for (int mm = 0; mm < 4; ++mm) PIN(af[c + 2][mm]);
            }
#pragma unroll
            for (int n5 = 0; n5 < 5; ++n5) {
                const u32x4 wf = fw[st & 1][n5];
                if (st + 1 < 20) rd(st + 1, n5);
                acc[n5] = mfma16(wf, af[c][m], acc[n5]);
                SBAR();
            }
        }
#pragma unroll
        for (int n5 = 0; n5 < 5; ++n5) {
            const u32x2 xv = *(const u32x2*)(x + tok * D + ct * 80 + n5 * 16 + g * 4);
            *(u32x2*)(hp + n5 * 1024) = (u32x2){pk2(acc[n5][0] + bf_lo(xv[0]), acc[n5][1] + bf_hi(xv[0])), pk2(acc[n5][2] + bf_lo(xv[1]), acc[n5][3] + bf_hi(xv[1]))};
        }
    }
    TS_END(DOWN ? 4 : 2, ws);
}

__global__ __launch_bounds__(256, 1) void ffn_up_kernel(const u16* __restrict__ wg, const u16* __restrict__ wu, char* ws) {
    extern __shared__ __attribute__((aligned(16))) char smem[];
    TS_BEGIN(3)
    constexpr int IMG = 14336 + 128, SLOTB = 2 * IMG;
    const int tid = threadIdx.x, lane = tid & 63, wid = __builtin_amdgcn_readfirstlane(tid >> 6), r32 = lane & 31, hf = lane >> 5;
    const int xcd = blockIdx.x & 7, idx = blockIdx.x >> 3;
    const int pm = (xcd >> 1) * 4 + (idx & 3), cj = (xcd & 1) * 8 + (idx >> 2), u0 = (27 * cj) >> 2;
    const int T = pm * 4 + wid;
    const unsigned lds0 = (unsigned)(uintptr_t)(LAS char*)smem;
    unsigned wo_[7];
#pragma unroll
    for (int i = 0; i < 7; ++i) { const int id = ((wid & 1) * 7 + i) * 64 + lane, row = id / 14, cc = id - row * 14; wo_[i] = (unsigned)(row * (FF * 2) + cc * 16 + 4096 - (i & 3) * 1024); }
    const char* wb = (const char*)((wid >> 1) ? wu : wg) + u0 * 32 - 4096;
    const char* hb = ws + WS_H + (size_t)T * 40 * 1024;
    u32x4 tf[40];
    auto issue_a = [&](int c) { glds4(wb + (size_t)c * 64 * (FF * 2), wo_[0], wo_[1], wo_[2], wo_[3], lds0 + (c % 5) * SLOTB + (wid >> 1) * IMG + (wid & 1) * 7168); };
    auto issue_b = [&](int c) { glds3(wb + (size_t)c * 64 * (FF * 2), wo_[4], wo_[5], wo_[6], lds0 + (c % 5) * SLOTB + (wid >> 1) * IMG + (wid & 1) * 7168 + 4096); };
    auto issue_t = [&](int c) {
        gload16<0>(tf[4 * c], hb + c * 4096, lane * 16); gload16<1024>(tf[4 * c + 1], hb + c * 4096, lane * 16);
        gload16<2048>(tf[4 * c + 2], hb + c * 4096, lane * 16); gload16<3072>(tf[4 * c + 3], hb + c * 4096, lane * 16);
    };
#pragma unroll
    for (int c = 0; c < 5; ++c) { issue_a(c); issue_b(c); issue_t(c); }
    LAS const char* sm = (LAS const char*)smem;
    int wa[7];
#pragma unroll
    for (int n = 0; n < 7; ++n) { int u = n + 2 * wid; u -= (u >= 7) ? 7 : 0; wa[n] = ((lane >> 4) & 1) * IMG + (8 * hf + ((lane >> 2) & 3)) * 224 + u * 32 + (lane & 3) * 8; }
    f32x16 acc[7] = {};
    float sx = 0.f, sxx = 0.f, sw0 = 0.f, sw1 = 0.f;
    u32x4 fw[2][7];
    auto rd = [&](int st, int n) {
        const int c = st >> 2, s = st & 3, so = (c % 5) * SLOTB, hi = (so / (2 * SLOTB)) * (2 * SLOTB);
        LAS const char* p = sm + (so - hi) + s * 16 * 224 + OPQ(wa[n], hi);
        fw[st & 1][n] = trfrag(p, p + 4 * 224);
    };
    wait_vm_bar<44>();
#pragma unroll
    for (int s = 0; s < 4; ++s) PIN(tf[s]);
#pragma unroll
    for (int n = 0; n < 7; ++n) rd(0, n);
    SBAR();
#pragma unroll
    for (int st = 0; st < 40; ++st) {
        const int c = st >> 2, s = st & 3;
        if (s == 3 && c < 9) {
            if (c == 0) wait_vm_bar_nl<33>(); else if (c <= 6) wait_vm_bar_nl<22>(); else if (c == 7) wait_vm_bar_nl<11>(); else wait_vm_bar_nl<0>();
#pragma unroll
            for (int ss = 0; ss < 4; ++ss) PIN(tf[4 * (c + 1) + ss]);
        }
        const u32x4 t = tf[st], wz0 = fw[st & 1][0], wz1 = fw[st & 1][1];
#pragma unroll
        for (int n = 0; n < 7; ++n) {
            const u32x4 wf = fw[st & 1][n];
            if (st + 1 < 40) rd(st + 1, n);
            acc[n] = mfma32(wf, t, acc[n]);
            if (n < 4) { sx = dot2(t[n], 0x3f803f80u, sx); sxx = dot2(t[n], t[n], sxx); }
            if (n >= 3) { sw0 = dot2(wz0[n - 3], 0x3f803f80u, sw0); sw1 = dot2(wz1[n - 3], 0x3f803f80u, sw1); }
            if (s == 3 && c >= 1 && c + 4 < 10) { if (n == 1) issue_a(c + 4); if (n == 3) issue_b(c + 4); if (n == 5) issue_t(c + 4); }
            SBAR();
        }
    }
    sx += swap32(sx); sxx += swap32(sxx); sw0 += swap32(sw0); sw1 += swap32(sw1);
    const float mean = sx * (1.f / D), var = fmaxf(sxx * (1.f / D) - mean * mean, 0.f) + LN_EPS, rstd = __builtin_amdgcn_rsqf(var);
    LAS float* st = (LAS float*)smem;
    if (hf == 0) { st[(2 * wid) * 32 + r32] = sw0; if (wid < 3) st[(2 * wid + 1) * 32 + r32] = sw1; }
    asm volatile("s_waitcnt lgkmcnt(0)\n\ts_barrier" ::: "memory");
    const float k1 = -2.f * 0.7978845608028654f * 1.4426950408889634f * rstd, k2 = k1 * 0.044715f * rstd * rstd, nm = -mean;
#pragma unroll
    for (int n = 0; n < 7; ++n) {
        int u = n + 2 * wid; u -= (u >= 7) ? 7 : 0;
        float o[8];
#pragma unroll
        for (int a = 0; a < 2; ++a) {
            const f32x4 cg = *(LAS const f32x4*)(st + u * 32 + 8 * a + 4 * hf), cu = *(LAS const f32x4*)(st + u * 32 + 16 + 8 * a + 4 * hf);
#pragma unroll
            for (int e = 0; e < 4; ++e) {
                const float G = fmaf(nm, cg[e], acc[n][4 * a + e]), U = fmaf(nm, cu[e], acc[n][8 + 4 * a + e]);
                const float ex = __builtin_amdgcn_exp2f(G * fmaf(G * G, k2, k1));
                o[4 * a + e] = G * U * __builtin_amdgcn_rcpf(fmaf(ex, var, var));
            }
        }
        *(u32x4*)(ws + WS_G + ((size_t)T * 108 + u0 + u) * 1024 + lane * 16) = (u32x4){pk2(o[0], o[1]), pk2(o[2], o[3]), pk2(o[4], o[5]), pk2(o[6], o[7])};
    }
    TS_END(3, ws);
}

extern "C" void kernel_launch(void* const* d_in, const int* in_sizes, int n_in, void* d_out, int out_size, void* d_ws, size_t ws_size, hipStream_t stream) {
    const u16* x = (const u16*)d_in[0]; const u16* wqkv = (const u16*)d_in[1]; const u16* wo = (const u16*)d_in[2];
    const u16* wg = (const u16*)d_in[4]; const u16* wu = (const u16*)d_in[5]; const u16* wd = (const u16*)d_in[6];
    char* ws = (char*)d_ws;
    (void)hipFuncSetAttribute((const void*)qkv_kernel, hipFuncAttributeMaxDynamicSharedMemorySize, 163840);
    (void)hipFuncSetAttribute((const void*)proj_kernel<false>, hipFuncAttributeMaxDynamicSharedMemorySize, 102400);
    (void)hipFuncSetAttribute((const void*)proj_kernel<true>, hipFuncAttributeMaxDynamicSharedMemorySize, 143360);
    (void)hipFuncSetAttribute((const void*)ffn_up_kernel, hipFuncAttributeMaxDynamicSharedMemorySize, 5 * 2 * (14336 + 128));
    qkv_kernel<<<240, 256, 163840, stream>>>(x, wqkv, ws);
    attn_kernel<ATTN_NW><<<256, ATTN_NW * 64, 0, stream>>>(ws);
    proj_kernel<false><<<256, 256, 102400, stream>>>(wo, x, ws, nullptr);
    ffn_up_kernel<<<256, 256, 5 * 2 * (14336 + 128), stream>>>(wg, wu, ws);
    proj_kernel<true><<<256, 256, 143360, stream>>>(wd, nullptr, ws, (u16*)d_out);
}
```

```cpp
#include <hip/hip_runtime.h>
#include <hip/hip_bf16.h>
#include <stdint.h>

typedef unsigned short u16;
typedef __bf16 bf16x2 __attribute__((ext_vector_type(2)));
typedef __bf16 bf16x8 __attribute__((ext_vector_type(8)));
typedef short s16x4 __attribute__((ext_vector_type(4)));
typedef float f32x2 __attribute__((ext_vector_type(2)));
typedef float f32x4 __attribute__((ext_vector_type(4)));
typedef float f32x16 __attribute__((ext_vector_type(16)));
typedef unsigned u32x2 __attribute__((ext_vector_type(2)));
typedef unsigned u32x4 __attribute__((ext_vector_type(4)));
typedef unsigned u32x16 __attribute__((ext_vector_type(16)));
#define LAS __attribute__((address_space(3)))
#define DI __device__ __forceinline__

constexpr int D = 640, DH = 20, FF = 1728;
constexpr unsigned TILE = 2560;
constexpr unsigned WS_FLAG = 4096;
constexpr unsigned WS_XS = 229376;
constexpr unsigned WS_CS = WS_XS + 32768;
constexpr unsigned WS_Q = 524288;
constexpr unsigned WS_K = WS_Q + 16 * 64 * TILE;
constexpr unsigned WS_V = WS_K + 16 * 64 * TILE;
constexpr unsigned WS_O = WS_V + 16 * 64 * TILE + 8192;
constexpr unsigned WS_H = WS_O + 64 * 40960;
constexpr unsigned WS_HS = WS_H + 64 * 40960;
constexpr unsigned WS_G = WS_HS + 131072;
constexpr unsigned WS_WU = WS_G + 64 * 108 * 1024;
constexpr unsigned WS_WD = WS_WU + 108 * 40960;
constexpr unsigned WS_WO = WS_WD + 8 * 54 * 5120 + 16384;
constexpr unsigned WS_END = WS_WO + 8 * 20 * 5120;
enum { F_QKV = 0, F_RT = 1, F_ATT = 2, F_WO = 3, F_UP = 4, F_UPE = 5 };
constexpr float LN_EPS = 1e-5f;
constexpr float QSCALE = 0.22360679774997896f * 1.4426950408889634f;
constexpr unsigned ONES = 0x3f803f80u;
constexpr int SPIN_CAP = 1 << 20;


DI unsigned pk2(float a, float b) { f32x2 p = {a, b}; return __builtin_bit_cast(unsigned, __builtin_convertvector(p, bf16x2)); }
DI float dot2(unsigned a, unsigned b, float c) { return __builtin_amdgcn_fdot2_f32_bf16(__builtin_bit_cast(bf16x2, a), __builtin_bit_cast(bf16x2, b), c, false); }
DI f32x16 mfma32(u32x4 a, u32x4 b, f32x16 c) { return __builtin_amdgcn_mfma_f32_32x32x16_bf16(__builtin_bit_cast(bf16x8, a), __builtin_bit_cast(bf16x8, b), c, 0, 0, 0); }
DI f32x16 mfma32k8(u32x2 a, u32x2 b, f32x16 c) { return __builtin_amdgcn_mfma_f32_32x32x8bf16_1k(__builtin_bit_cast(s16x4, a), __builtin_bit_cast(s16x4, b), c, 0, 0, 0); }
DI f32x4 mfma16(u32x4 a, u32x4 b, f32x4 c) { return __builtin_amdgcn_mfma_f32_16x16x32_bf16(__builtin_bit_cast(bf16x8, a), __builtin_bit_cast(bf16x8, b), c, 0, 0, 0); }
DI u32x2 trread(LAS const char* p) { return __builtin_bit_cast(u32x2, __builtin_amdgcn_ds_read_tr16_b64_v4i16((LAS s16x4*)p)); }
DI u32x4 trfrag(LAS const char* p0, LAS const char* p1) { u32x2 a = trread(p0), b = trread(p1); return (u32x4){a[0], a[1], b[0], b[1]}; }
DI float bf_lo(unsigned w) { return __uint_as_float(w << 16); }
DI float bf_hi(unsigned w) { return __uint_as_float(w & 0xffff0000u); }
template <int CTRL> DI float dpp_add(float v) { return v + __int_as_float(__builtin_amdgcn_update_dpp(0, __float_as_int(v), CTRL, 0xf, 0xf, true)); }
DI float sum_x8(float v) { return dpp_add<0x128>(v); }
DI float sum_row(float v) { return dpp_add<0x140>(dpp_add<0x141>(dpp_add<0x4E>(dpp_add<0xB1>(v)))); }
DI float sum_x16(float v) { auto r = __builtin_amdgcn_permlane16_swap(__float_as_uint(v), __float_as_uint(v), false, false); return __uint_as_float(r[0]) + __uint_as_float(r[1]); }
DI float sum_x32(float v) { auto r = __builtin_amdgcn_permlane32_swap(__float_as_uint(v), __float_as_uint(v), false, false); return __uint_as_float(r[0]) + __uint_as_float(r[1]); }
#define PIN(x) asm volatile("" : "+v"(x))
#define SBAR() __builtin_amdgcn_sched_barrier(0)
template <int I> struct IC { static constexpr int v = I; };
template <int I, int N, class F> DI void sfor_(F& f) { if constexpr (I < N) { f(IC<I>{}); sfor_<I + 1, N>(f); } }
template <int N, class F> DI void sfor(F f) { sfor_<0, N>(f); }
template <int N> DI void wait_vm() { asm volatile("s_waitcnt vmcnt(%0)" :: "i"(N) : "memory"); }
template <int N> DI void wait_vm_bar() { asm volatile("s_waitcnt vmcnt(%0)\n\ts_barrier" :: "i"(N) : "memory"); }
DI void bar() { asm volatile("s_barrier" ::: "memory"); }
DI void bar_l() { asm volatile("s_waitcnt lgkmcnt(0)\n\ts_barrier" ::: "memory"); }
DI int opq_add(int v, int k) { int r = v + k; asm("" : "+v"(r)); return r; }
#define OPQ(v, hi) ((hi) ? opq_add((v), (hi)) : (v))

template <bool SC> DI void glds1(const void* gbase, unsigned v0, unsigned lds) {
    unsigned keep;
    if (SC) asm volatile("s_mov_b32 %0, m0\n\ts_mov_b32 m0, %3\n\ts_nop 0\n\tglobal_load_lds_dwordx4 %1, %2 sc1\n\ts_mov_b32 m0, %0" : "=&s"(keep) : "v"(v0), "s"(gbase), "s"(lds) : "memory");
    else asm volatile("s_mov_b32 %0, m0\n\ts_mov_b32 m0, %3\n\ts_nop 0\n\tglobal_load_lds_dwordx4 %1, %2\n\ts_mov_b32 m0, %0" : "=&s"(keep) : "v"(v0), "s"(gbase), "s"(lds) : "memory");
}
DI void glds4(const void* gbase, unsigned v0, unsigned v1, unsigned v2, unsigned v3, unsigned lds) {
    unsigned keep;
    asm volatile("s_mov_b32 %0, m0\n\ts_mov_b32 m0, %6\n\ts_nop 0\n\t"
                 "global_load_lds_dwordx4 %1, %5\n\tglobal_load_lds_dwordx4 %2, %5 offset:1024\n\t"
                 "global_load_lds_dwordx4 %3, %5 offset:2048\n\tglobal_load_lds_dwordx4 %4, %5 offset:3072\n\ts_mov_b32 m0, %0"
                 : "=&s"(keep) : "v"(v0), "v"(v1), "v"(v2), "v"(v3), "s"(gbase), "s"(lds) : "memory");
}
template <int N> DI void glds_lin(const void* gbase, unsigned v0, unsigned lds) {
    unsigned keep;
    if (N == 4) asm volatile("s_mov_b32 %0, m0\n\ts_mov_b32 m0, %3\n\ts_nop 0\n\tglobal_load_lds_dwordx4 %1, %2 sc1\n\tglobal_load_lds_dwordx4 %1, %2 offset:1024 sc1\n\t"
                             "global_load_lds_dwordx4 %1, %2 offset:2048 sc1\n\tglobal_load_lds_dwordx4 %1, %2 offset:3072 sc1\n\ts_mov_b32 m0, %0" : "=&s"(keep) : "v"(v0), "s"(gbase), "s"(lds) : "memory");
    if (N == 2) asm volatile("s_mov_b32 %0, m0\n\ts_mov_b32 m0, %3\n\ts_nop 0\n\tglobal_load_lds_dwordx4 %1, %2 sc1\n\tglobal_load_lds_dwordx4 %1, %2 offset:1024 sc1\n\ts_mov_b32 m0, %0"
                             : "=&s"(keep) : "v"(v0), "s"(gbase), "s"(lds) : "memory");
    if (N == 1) glds1<true>(gbase, v0, lds);
}
DI void glds_touch(const void* gbase, unsigned v0, unsigned lds) {
    unsigned keep;
    asm volatile("s_mov_b32 %0, m0\n\ts_mov_b32 m0, %3\n\ts_nop 0\n\tglobal_load_lds_dword %1, %2 sc1\n\ts_mov_b32 m0, %0" : "=&s"(keep) : "v"(v0), "s"(gbase), "s"(lds) : "memory");
}
template <int OFF, bool SC> DI void gld16(u32x4& dst, const void* gbase, unsigned voff) {
    if (SC) asm volatile("global_load_dwordx4 %0, %1, %2 offset:%3 sc1" : "=v"(dst) : "v"(voff), "s"(gbase), "i"(OFF) : "memory");
    else asm volatile("global_load_dwordx4 %0, %1, %2 offset:%3" : "=v"(dst) : "v"(voff), "s"(gbase), "i"(OFF) : "memory");
}
template <int OFF, bool SC> DI void gld8(u32x2& dst, const void* gbase, unsigned voff) {
    if (SC) asm volatile("global_load_dwordx2 %0, %1, %2 offset:%3 sc1" : "=v"(dst) : "v"(voff), "s"(gbase), "i"(OFF) : "memory");
    else asm volatile("global_load_dwordx2 %0, %1, %2 offset:%3" : "=v"(dst) : "v"(voff), "s"(gbase), "i"(OFF) : "memory");
}
DI void gld4sc(unsigned& dst, const void* gbase, unsigned voff) { asm volatile("global_load_dword %0, %1, %2 sc1" : "=v"(dst) : "v"(voff), "s"(gbase) : "memory"); }
DI void st16(char* base, unsigned voff, u32x4 v) { asm volatile("global_store_dwordx4 %0, %1, %2 sc1\n\ts_nop 1" :: "v"(voff), "v"(v), "s"(base) : "memory"); }
DI void st16_if(char* base, unsigned voff, u32x4 v, unsigned long long mask) {
    asm volatile("s_mov_b64 exec, %3\n\tglobal_store_dwordx4 %0, %1, %2 sc1\n\ts_mov_b64 exec, -1\n\ts_nop 0" :: "v"(voff), "v"(v), "s"(base), "s"(mask) : "memory");
}
DI void st8(char* base, unsigned voff, u32x2 v) { asm volatile("global_store_dwordx2 %0, %1, %2 sc1" :: "v"(voff), "v"(v), "s"(base) : "memory"); }
DI void st4(char* base, unsigned voff, unsigned v) { asm volatile("global_store_dword %0, %1, %2 sc1" :: "v"(voff), "v"(v), "s"(base) : "memory"); }
DI unsigned uni(unsigned v) { return __builtin_amdgcn_readfirstlane(v); }
DI unsigned flag_off(int kind, int i) { return WS_FLAG + (unsigned)(kind * 256 + i) * 128u; }
DI void publish(char* ws, unsigned off, unsigned tag, int wid, int who) {
    unsigned a = off, t = tag;
    asm volatile("s_cmp_lg_u32 %3, %4\n\ts_waitcnt vmcnt(0) lgkmcnt(0)\n\ts_barrier\n\ts_cbranch_scc1 1f\n\ts_mov_b64 exec, 1\n\tglobal_store_dword %0, %1, %2 sc1\n\ts_mov_b64 exec, -1\n\t1:"
                 :: "v"(a), "v"(t), "s"(ws), "s"(wid), "s"(who) : "scc", "memory");
}
DI void poll(const char* ws, unsigned off, unsigned tag) {
#pragma unroll 1
    for (int spins = 0; spins < SPIN_CAP; ++spins) {
        unsigned v; asm volatile("global_load_dword %0, %1, %2 sc1\n\ts_waitcnt vmcnt(0)" : "=&v"(v) : "v"(off), "s"(ws) : "memory");
        if (__all(v == tag)) break;
        __builtin_amdgcn_s_sleep(1);
    }
}

constexpr int L_ST = 32768;
constexpr int L_CTL = 49152;
constexpr int L_WO = 61440;

DI unsigned split_bf16(float v) { const unsigned hi = pk2(v, 0.f) & 0xffffu; return hi | (pk2(v - bf_lo(hi), 0.f) << 16); }
DI f32x4 mfma16k16(u32x2 a, u32x2 b, f32x4 c) { return __builtin_amdgcn_mfma_f32_16x16x16bf16_1k(__builtin_bit_cast(s16x4, a), __builtin_bit_cast(s16x4, b), c, 0, 0, 0); }
template <bool ISV, class F>
DI void qkv_compute(char* ws, LAS char* sm, int pm, int pn, int wid, int lane, F side) {
    side(IC<-1>{});
    const int wr = wid >> 1, wc = wid & 1, g = lane >> 4, c16 = lane & 15;
    int xs[2], wl[2];
#pragma unroll
    for (int s = 0; s < 2; ++s) {
        xs[s] = wr * 8192 + c16 * 128 + (((4 * s + g) ^ (c16 >> 1)) << 4); PIN(xs[s]);
        wl[s] = 16384 + wc * 512 + g * 2048 + ((lane >> 2) & 3) * 64 + ((s ^ (g & 1)) * 32) + (lane & 3) * 8; PIN(wl[s]);
    }
    unsigned dst[4];
    const bool isq = pn < 5;
#pragma unroll
    for (int j = 0; j < 4; ++j) {
        if (ISV) { const int col = (pn - 10) * 128 + wc * 64 + j * 16 + c16, hp = col / 40; dst[j] = WS_V + (unsigned)(hp * 64 + pm * 4 + 2 * wr) * TILE + g * 640 + (col - hp * 40) * 16; }
        else {
            const int ch = (isq ? pn : pn - 5) * 16 + wc * 8 + j * 2 + (g >> 1), hp = ch / 5, row = isq ? lane & 31 : (lane & 7) | ((lane & 8) << 1) | ((lane & 16) >> 1);
            dst[j] = (isq ? WS_Q : WS_K) + (unsigned)(hp * 64 + pm * 4 + 2 * wr) * TILE + (ch - hp * 5) * 512 + row * 16;
        }
        PIN(dst[j]);
    }
    int ta = L_ST + 512 + (wr * 64 + c16) * 8, ca = g ? L_ST + 2560 : L_ST + 1536 + (wc * 64 + c16) * 8, ra = L_ST + (wr * 64 + (ISV ? 4 * g : c16)) * 4;
    PIN(ta); PIN(ca); PIN(ra);
    f32x4 acc[4][4] = {};
    u32x4 fx[2][4], fw[2][4];
    auto rd = [&](int st, int which) {
        const int c = st >> 1, s = st & 1, so = (c % 5) * 32768, hi = so & ~65535;
        LAS const char* p = sm + (so - hi);
        if (which == 0 || which > 4) { const int i = which ? which - 4 : 0; fx[st & 1][i] = *(LAS const u32x4*)(p + OPQ(xs[s], hi) + i * 2048); }
        else { const int j = which - 1; LAS const char* q = p + OPQ(wl[j & 1], hi) + s * 8192 + (j >> 1) * 256; fw[st & 1][j] = trfrag(q, q + 1024); }
    };
    bar();
#pragma unroll
    for (int w = 0; w < 8; ++w) rd(0, w);
    SBAR();
    sfor<160>([&](auto GG) {
        constexpr int st = GG.v >> 3, w = GG.v & 7, i = w >> 1, j = (w & 1) * 2, bf = st & 1;
        if (w == 0 && bf == 1 && st < 19) bar();
        if (st == 19 && w == 7) bar();
        const u32x4 x = fx[bf][i], w0 = fw[bf][j], w1 = fw[bf][j + 1];
        acc[i][j] = ISV ? mfma16(x, w0, acc[i][j]) : mfma16(w0, x, acc[i][j]);
        if (st < 19) rd(st + 1, w);
        acc[i][j + 1] = ISV ? mfma16(x, w1, acc[i][j + 1]) : mfma16(w1, x, acc[i][j + 1]);
        side(GG); SBAR();
    });
    u32x2 tm[4], cm[4]; f32x4 rv[4]; float rs[4];
#pragma unroll
    for (int i = 0; i < 4; ++i) { tm[i] = *(LAS const u32x2*)(sm + ta + i * 128); cm[i] = *(LAS const u32x2*)(sm + ca + i * 128); }
#pragma unroll
    for (int i = 0; i < 4; ++i) {
        if (ISV) rv[i] = *(LAS const f32x4*)(sm + ra + i * 64);
        else { rs[i] = *(LAS const float*)(sm + ra + i * 64); if (isq) rs[i] *= QSCALE; rv[i] = (f32x4){rs[i], rs[i], rs[i], rs[i]}; }
    }
#pragma unroll
    for (int e = 0; e < 16; ++e) { const int i = (e >> 3) * 2 + (e & 1), j = (e >> 1) & 3; acc[i][j] = ISV ? mfma16k16(tm[i], cm[j], acc[i][j]) : mfma16k16(cm[j], tm[i], acc[i][j]); }
#pragma unroll
    for (int t = 0; t < 2; ++t)
#pragma unroll
        for (int j = 0; j < 4; ++j) {
            const f32x4 a = acc[2 * t][j] * rv[2 * t], b = acc[2 * t + 1][j] * rv[2 * t + 1];
            u32x4 o = {pk2(a[0], a[1]), pk2(a[2], a[3]), pk2(b[0], b[1]), pk2(b[2], b[3])};
            if (!ISV) {
                const auto s0 = __builtin_amdgcn_permlane16_swap(o[0], o[2], false, false), s1 = __builtin_amdgcn_permlane16_swap(o[1], o[3], false, false);
                o = (u32x4){s0[0], s1[0], s0[1], s1[1]};
            }
            st16(ws + t * TILE, dst[j], o);
        }
}

#define ATTN_ASM \
    "v_mov_b32 v128, 0\n\t" \
    "v_mov_b32 v129, 0\n\t" \
    "v_mov_b32 v130, 0\n\t" \
    "v_mov_b32 v131, 0\n\t" \
    "v_mov_b32 v132, 0\n\t" \
    "v_mov_b32 v133, 0\n\t" \
    "v_mov_b32 v134, 0\n\t" \
    "v_mov_b32 v135, 0\n\t" \
    "v_mov_b32 v162, 0\n\t" \
    "v_mov_b32 v163, 0\n\t" \
    "v_mov_b32 v154, 0\n\t" \
    "v_mov_b32 v155, 0\n\t" \
    "v_mov_b32 v156, 0\n\t" \
    "v_mov_b32 v157, 0\n\t" \
    "v_mov_b32 v158, 0\n\t" \
    "v_mov_b32 v159, 0\n\t" \
    "v_mov_b32 v160, 0\n\t" \
    "v_mov_b32 v161, 0\n\t" \
    "v_mov_b32 v104, 0\n\t" \
    "v_mov_b32 v105, 0\n\t" \
    "v_mov_b32 v106, 0\n\t" \
    "v_mov_b32 v107, 0\n\t" \
    "v_mov_b32 v108, 0\n\t" \
    "v_mov_b32 v109, 0\n\t" \
    "v_mov_b32 v110, 0\n\t" \
    "v_mov_b32 v111, 0\n\t" \
    "v_mov_b64 v[0:1], 0\n\t" \
    "v_mov_b64 v[2:3], 0\n\t" \
    "v_mov_b64 v[4:5], 0\n\t" \
    "v_mov_b64 v[6:7], 0\n\t" \
    "v_mov_b64 v[8:9], 0\n\t" \
    "v_mov_b64 v[10:11], 0\n\t" \
    "v_mov_b64 v[12:13], 0\n\t" \
    "v_mov_b64 v[14:15], 0\n\t" \
    "v_mov_b64 v[16:17], 0\n\t" \
    "v_mov_b64 v[18:19], 0\n\t" \
    "v_mov_b64 v[20:21], 0\n\t" \
    "v_mov_b64 v[22:23], 0\n\t" \
    "v_mov_b64 v[24:25], 0\n\t" \
    "v_mov_b64 v[26:27], 0\n\t" \
    "v_mov_b64 v[28:29], 0\n\t" \
    "v_mov_b64 v[30:31], 0\n\t" \
    "v_mov_b64 v[32:33], 0\n\t" \
    "v_mov_b64 v[34:35], 0\n\t" \
    "v_mov_b64 v[36:37], 0\n\t" \
    "v_mov_b64 v[38:39], 0\n\t" \
    "v_mov_b64 v[40:41], 0\n\t" \
    "v_mov_b64 v[42:43], 0\n\t" \
    "v_mov_b64 v[44:45], 0\n\t" \
    "v_mov_b64 v[46:47], 0\n\t" \
    "v_mov_b64 v[48:49], 0\n\t" \
    "v_mov_b64 v[50:51], 0\n\t" \
    "v_mov_b64 v[52:53], 0\n\t" \
    "v_mov_b64 v[54:55], 0\n\t" \
    "v_mov_b64 v[56:57], 0\n\t" \
    "v_mov_b64 v[58:59], 0\n\t" \
    "v_mov_b64 v[60:61], 0\n\t" \
    "v_mov_b64 v[62:63], 0\n\t" \
    "v_mov_b32 v173, 2\n\t" \
    "s_mov_b32 s48, 4\n\t" \
    "s_mov_b32 s49, 0x1000000\n\t" \
    "s_mov_b32 s56, 0x2b8cbccc\n\t" \
    "s_mov_b32 s57, 0x7b4097ce\n\t" \
    "s_barrier\n\t" \
    "global_load_dwordx4 v[150:153], v164, s[46:47] sc1\n\t" \
    "global_load_dwordx4 v[112:115], v164, s[44:45] offset:0 sc1\n\t" \
    "global_load_dwordx4 v[116:119], v164, s[44:45] offset:2560 sc1\n\t" \
    "global_load_dwordx4 v[120:123], v164, s[42:43] offset:0 sc1\n\t" \
    "global_load_dwordx4 v[124:127], v164, s[42:43] offset:2560 sc1\n\t" \
    "s_mov_b32 exec_hi, 0\n\t" \
    "global_load_dwordx2 v[162:163], v165, s[46:47] sc1\n\t" \
    "global_load_dwordx2 v[128:129], v165, s[44:45] offset:0 sc1\n\t" \
    "global_load_dwordx2 v[130:131], v165, s[44:45] offset:2560 sc1\n\t" \
    "global_load_dwordx2 v[132:133], v165, s[42:43] offset:0 sc1\n\t" \
    "global_load_dwordx2 v[134:135], v165, s[42:43] offset:2560 sc1\n\t" \
    "s_mov_b32 exec_hi, -1\n\t" \
    "s_waitcnt vmcnt(8)\n\t" \
    "v_mfma_f32_32x32x16_bf16 v[176:191], v[150:153], v[112:115], 0\n\t" \
    "s_waitcnt vmcnt(7)\n\t" \
    "v_mfma_f32_32x32x16_bf16 v[192:207], v[150:153], v[116:119], 0\n\t" \
    "s_waitcnt vmcnt(6)\n\t" \
    "v_mfma_f32_32x32x16_bf16 v[208:223], v[150:153], v[120:123], 0\n\t" \
    "s_waitcnt vmcnt(5)\n\t" \
    "v_mfma_f32_32x32x16_bf16 v[224:239], v[150:153], v[124:127], 0\n\t" \
    "s_waitcnt vmcnt(3)\n\t" \
    "v_mfma_f32_32x32x8_bf16 v[176:191], v[162:163], v[128:129], v[176:191]\n\t" \
    "s_waitcnt vmcnt(2)\n\t" \
    "v_mfma_f32_32x32x8_bf16 v[192:207], v[162:163], v[130:131], v[192:207]\n\t" \
    "s_waitcnt vmcnt(1)\n\t" \
    "v_mfma_f32_32x32x8_bf16 v[208:223], v[162:163], v[132:133], v[208:223]\n\t" \
    "s_waitcnt vmcnt(0)\n\t" \
    "v_mfma_f32_32x32x8_bf16 v[224:239], v[162:163], v[134:135], v[224:239]\n\t" \
    "s_nop 5\n\t" \
    "v_max3_f32 v80, v176, v177, v178\n\t" \
    "v_max3_f32 v81, v179, v180, v181\n\t" \
    "v_max3_f32 v80, v80, v182, v183\n\t" \
    "v_max3_f32 v81, v81, v184, v185\n\t" \
    "v_max3_f32 v80, v80, v186, v187\n\t" \
    "v_max3_f32 v81, v81, v188, v189\n\t" \
    "v_max3_f32 v80, v80, v190, v191\n\t" \
    "v_max3_f32 v82, v192, v193, v194\n\t" \
    "v_max3_f32 v83, v195, v196, v197\n\t" \
    "v_max3_f32 v82, v82, v198, v199\n\t" \
    "v_max3_f32 v83, v83, v200, v201\n\t" \
    "v_max3_f32 v82, v82, v202, v203\n\t" \
    "v_max3_f32 v83, v83, v204, v205\n\t" \
    "v_max3_f32 v82, v82, v206, v207\n\t" \
    "v_max3_f32 v84, v208, v209, v210\n\t" \
    "v_max3_f32 v85, v211, v212, v213\n\t" \
    "v_max3_f32 v84, v84, v214, v215\n\t" \
    "v_max3_f32 v85, v85, v216, v217\n\t" \
    "v_max3_f32 v84, v84, v218, v219\n\t" \
    "v_max3_f32 v85, v85, v220, v221\n\t" \
    "v_max3_f32 v84, v84, v222, v223\n\t" \
    "v_max3_f32 v86, v224, v225, v226\n\t" \
    "v_max3_f32 v87, v227, v228, v229\n\t" \
    "v_max3_f32 v86, v86, v230, v231\n\t" \
    "v_max3_f32 v87, v87, v232, v233\n\t" \
    "v_max3_f32 v86, v86, v234, v235\n\t" \
    "v_max3_f32 v87, v87, v236, v237\n\t" \
    "v_max3_f32 v86, v86, v238, v239\n\t" \
    "s_barrier\n\t" \
    "ds_read_b128 v[136:139], v166 offset:0\n\t" \
    "ds_read_b64 v[148:149], v167 offset:0\n\t" \
    "ds_read_b128 v[140:143], v168 offset:0\n\t" \
    "ds_read_b128 v[144:147], v169 offset:0\n\t" \
    "v_max_f32 v80, v80, v81\n\t" \
    "v_max_f32 v82, v82, v83\n\t" \
    "v_max_f32 v84, v84, v85\n\t" \
    "v_max_f32 v86, v86, v87\n\t" \
    "s_mov_b32 exec_lo, 0\n\t" \
    "v_cvt_pk_bf16_f32 v128, -v80, 0\n\t" \
    "v_cvt_pk_bf16_f32 v130, -v82, 0\n\t" \
    "v_cvt_pk_bf16_f32 v132, -v84, 0\n\t" \
    "v_cvt_pk_bf16_f32 v134, -v86, 0\n\t" \
    "s_mov_b32 exec_lo, -1\n\t" \
    "s_waitcnt lgkmcnt(0)\n\t" \
    "v_mfma_f32_32x32x16_bf16 v[64:79], v[136:139], v[112:115], 0\n\t" \
    "v_mfma_f32_32x32x8_bf16 v[64:79], v[148:149], v[128:129], v[64:79]\n\t" \
    "s_nop 10\n\t" \
    "1:\n\t" \
    "v_mfma_f32_32x32x16_bf16 v[80:95], v[136:139], v[116:119], 0\n\t" \
    "v_exp_f32 v64, v64\n\t" \
    "v_exp_f32 v65, v65\n\t" \
    "v_exp_f32 v66, v66\n\t" \
    "v_exp_f32 v67, v67\n\t" \
    "v_cvt_pk_bf16_f32 v96, v64, v65\n\t" \
    "v_exp_f32 v68, v68\n\t" \
    "v_mfma_f32_32x32x8_bf16 v[80:95], v[148:149], v[130:131], v[80:95]\n\t" \
    "v_exp_f32 v69, v69\n\t" \
    "v_cvt_pk_bf16_f32 v97, v66, v67\n\t" \
    "v_exp_f32 v70, v70\n\t" \
    "v_exp_f32 v71, v71\n\t" \
    "v_cvt_pk_bf16_f32 v98, v68, v69\n\t" \
    "v_exp_f32 v72, v72\n\t" \
    "v_mfma_f32_32x32x16_bf16 v[48:63], v[154:157], v[104:107], v[48:63]\n\t" \
    "v_exp_f32 v73, v73\n\t" \
    "v_cvt_pk_bf16_f32 v99, v70, v71\n\t" \
    "v_exp_f32 v74, v74\n\t" \
    "v_exp_f32 v75, v75\n\t" \
    "v_cvt_pk_bf16_f32 v100, v72, v73\n\t" \
    "v_exp_f32 v76, v76\n\t" \
    "v_mfma_f32_32x32x16_bf16 v[48:63], v[158:161], v[108:111], v[48:63]\n\t" \
    "v_exp_f32 v77, v77\n\t" \
    "v_cvt_pk_bf16_f32 v101, v74, v75\n\t" \
    "v_exp_f32 v78, v78\n\t" \
    "v_exp_f32 v79, v79\n\t" \
    "v_cvt_pk_bf16_f32 v102, v76, v77\n\t" \
    "v_cvt_pk_bf16_f32 v103, v78, v79\n\t" \
    "v_mfma_f32_32x32x16_bf16 v[64:79], v[136:139], v[120:123], 0\n\t" \
    "ds_read_b128 v[150:153], v166 offset:3072\n\t" \
    "ds_read_b64 v[162:163], v167 offset:3072\n\t" \
    "ds_read_b128 v[154:157], v168 offset:3072\n\t" \
    "ds_read_b128 v[158:161], v169 offset:3072\n\t" \
    "ds_read_b32 v172, v170\n\t" \
    "v_exp_f32 v80, v80\n\t" \
    "v_exp_f32 v81, v81\n\t" \
    "v_exp_f32 v82, v82\n\t" \
    "v_exp_f32 v83, v83\n\t" \
    "v_cvt_pk_bf16_f32 v104, v80, v81\n\t" \
    "v_exp_f32 v84, v84\n\t" \
    "v_mfma_f32_32x32x8_bf16 v[64:79], v[148:149], v[132:133], v[64:79]\n\t" \
    "v_exp_f32 v85, v85\n\t" \
    "v_cvt_pk_bf16_f32 v105, v82, v83\n\t" \
    "v_exp_f32 v86, v86\n\t" \
    "v_exp_f32 v87, v87\n\t" \
    "v_cvt_pk_bf16_f32 v106, v84, v85\n\t" \
    "v_exp_f32 v88, v88\n\t" \
    "v_mfma_f32_32x32x16_bf16 v[0:15], v[140:143], v[96:99], v[0:15]\n\t" \
    "v_exp_f32 v89, v89\n\t" \
    "v_cvt_pk_bf16_f32 v107, v86, v87\n\t" \
    "v_exp_f32 v90, v90\n\t" \
    "v_exp_f32 v91, v91\n\t" \
    "v_cvt_pk_bf16_f32 v108, v88, v89\n\t" \
    "v_exp_f32 v92, v92\n\t" \
    "v_mfma_f32_32x32x16_bf16 v[0:15], v[144:147], v[100:103], v[0:15]\n\t" \
    "v_exp_f32 v93, v93\n\t" \
    "v_cvt_pk_bf16_f32 v109, v90, v91\n\t" \
    "v_exp_f32 v94, v94\n\t" \
    "v_exp_f32 v95, v95\n\t" \
    "v_cvt_pk_bf16_f32 v110, v92, v93\n\t" \
    "v_cvt_pk_bf16_f32 v111, v94, v95\n\t" \
    "v_mfma_f32_32x32x16_bf16 v[80:95], v[136:139], v[124:127], 0\n\t" \
    "v_exp_f32 v64, v64\n\t" \
    "v_exp_f32 v65, v65\n\t" \
    "v_exp_f32 v66, v66\n\t" \
    "v_exp_f32 v67, v67\n\t" \
    "v_cvt_pk_bf16_f32 v96, v64, v65\n\t" \
    "v_exp_f32 v68, v68\n\t" \
    "v_mfma_f32_32x32x8_bf16 v[80:95], v[148:149], v[134:135], v[80:95]\n\t" \
    "v_exp_f32 v69, v69\n\t" \
    "v_cvt_pk_bf16_f32 v97, v66, v67\n\t" \
    "v_exp_f32 v70, v70\n\t" \
    "v_exp_f32 v71, v71\n\t" \
    "v_cvt_pk_bf16_f32 v98, v68, v69\n\t" \
    "v_exp_f32 v72, v72\n\t" \
    "v_mfma_f32_32x32x16_bf16 v[16:31], v[140:143], v[104:107], v[16:31]\n\t" \
    "v_exp_f32 v73, v73\n\t" \
    "v_cvt_pk_bf16_f32 v99, v70, v71\n\t" \
    "v_exp_f32 v74, v74\n\t" \
    "v_exp_f32 v75, v75\n\t" \
    "v_cvt_pk_bf16_f32 v100, v72, v73\n\t" \
    "v_exp_f32 v76, v76\n\t" \
    "v_mfma_f32_32x32x16_bf16 v[16:31], v[144:147], v[108:111], v[16:31]\n\t" \
    "v_exp_f32 v77, v77\n\t" \
    "v_cvt_pk_bf16_f32 v101, v74, v75\n\t" \
    "v_exp_f32 v78, v78\n\t" \
    "v_exp_f32 v79, v79\n\t" \
    "v_cvt_pk_bf16_f32 v102, v76, v77\n\t" \
    "v_cvt_pk_bf16_f32 v103, v78, v79\n\t" \
    "s_waitcnt lgkmcnt(0)\n\t" \
    "v_mfma_f32_32x32x16_bf16 v[64:79], v[150:153], v[112:115], 0\n\t" \
    "ds_write_b32 v171, v173\n\t" \
    "v_add_u32 v173, 2, v173\n\t" \
    "v_exp_f32 v80, v80\n\t" \
    "v_exp_f32 v81, v81\n\t" \
    "v_exp_f32 v82, v82\n\t" \
    "v_exp_f32 v83, v83\n\t" \
    "v_cvt_pk_bf16_f32 v104, v80, v81\n\t" \
    "v_exp_f32 v84, v84\n\t" \
    "v_mfma_f32_32x32x8_bf16 v[64:79], v[162:163], v[128:129], v[64:79]\n\t" \
    "v_exp_f32 v85, v85\n\t" \
    "v_cvt_pk_bf16_f32 v105, v82, v83\n\t" \
    "v_exp_f32 v86, v86\n\t" \
    "v_exp_f32 v87, v87\n\t" \
    "v_cvt_pk_bf16_f32 v106, v84, v85\n\t" \
    "v_exp_f32 v88, v88\n\t" \
    "v_mfma_f32_32x32x16_bf16 v[32:47], v[140:143], v[96:99], v[32:47]\n\t" \
    "v_exp_f32 v89, v89\n\t" \
    "v_cvt_pk_bf16_f32 v107, v86, v87\n\t" \
    "v_exp_f32 v90, v90\n\t" \
    "v_exp_f32 v91, v91\n\t" \
    "v_cvt_pk_bf16_f32 v108, v88, v89\n\t" \
    "v_exp_f32 v92, v92\n\t" \
    "v_mfma_f32_32x32x16_bf16 v[32:47], v[144:147], v[100:103], v[32:47]\n\t" \
    "v_exp_f32 v93, v93\n\t" \
    "v_cvt_pk_bf16_f32 v109, v90, v91\n\t" \
    "v_exp_f32 v94, v94\n\t" \
    "v_exp_f32 v95, v95\n\t" \
    "v_cvt_pk_bf16_f32 v110, v92, v93\n\t" \
    "v_cvt_pk_bf16_f32 v111, v94, v95\n\t" \
    "v_mfma_f32_32x32x16_bf16 v[80:95], v[150:153], v[116:119], 0\n\t" \
    "v_exp_f32 v64, v64\n\t" \
    "v_exp_f32 v65, v65\n\t" \
    "v_exp_f32 v66, v66\n\t" \
    "v_exp_f32 v67, v67\n\t" \
    "v_cvt_pk_bf16_f32 v96, v64, v65\n\t" \
    "v_exp_f32 v68, v68\n\t" \
    "v_mfma_f32_32x32x8_bf16 v[80:95], v[162:163], v[130:131], v[80:95]\n\t" \
    "v_exp_f32 v69, v69\n\t" \
    "v_cvt_pk_bf16_f32 v97, v66, v67\n\t" \
    "v_exp_f32 v70, v70\n\t" \
    "v_exp_f32 v71, v71\n\t" \
    "v_cvt_pk_bf16_f32 v98, v68, v69\n\t" \
    "v_exp_f32 v72, v72\n\t" \
    "v_mfma_f32_32x32x16_bf16 v[48:63], v[140:143], v[104:107], v[48:63]\n\t" \
    "v_exp_f32 v73, v73\n\t" \
    "v_cvt_pk_bf16_f32 v99, v70, v71\n\t" \
    "v_exp_f32 v74, v74\n\t" \
    "v_exp_f32 v75, v75\n\t" \
    "v_cvt_pk_bf16_f32 v100, v72, v73\n\t" \
    "v_cmp_le_u32 vcc, v173, v172\n\t" \
    "v_exp_f32 v76, v76\n\t" \
    "v_mfma_f32_32x32x16_bf16 v[48:63], v[144:147], v[108:111], v[48:63]\n\t" \
    "v_exp_f32 v77, v77\n\t" \
    "v_cvt_pk_bf16_f32 v101, v74, v75\n\t" \
    "v_exp_f32 v78, v78\n\t" \
    "v_exp_f32 v79, v79\n\t" \
    "v_cvt_pk_bf16_f32 v102, v76, v77\n\t" \
    "v_cvt_pk_bf16_f32 v103, v78, v79\n\t" \
    "v_mfma_f32_32x32x16_bf16 v[64:79], v[150:153], v[120:123], 0\n\t" \
    "s_cbranch_vccz 10f\n\t" \
    "11:\n\t" \
    "ds_read_b128 v[136:139], v166 offset:6144\n\t" \
    "ds_read_b64 v[148:149], v167 offset:6144\n\t" \
    "ds_read_b128 v[140:143], v168 offset:6144\n\t" \
    "ds_read_b128 v[144:147], v169 offset:6144\n\t" \
    "v_exp_f32 v80, v80\n\t" \
    "v_exp_f32 v81, v81\n\t" \
    "v_exp_f32 v82, v82\n\t" \
    "v_exp_f32 v83, v83\n\t" \
    "v_cvt_pk_bf16_f32 v104, v80, v81\n\t" \
    "v_exp_f32 v84, v84\n\t" \
    "v_mfma_f32_32x32x8_bf16 v[64:79], v[162:163], v[132:133], v[64:79]\n\t" \
    "v_exp_f32 v85, v85\n\t" \
    "v_cvt_pk_bf16_f32 v105, v82, v83\n\t" \
    "v_exp_f32 v86, v86\n\t" \
    "v_exp_f32 v87, v87\n\t" \
    "v_cvt_pk_bf16_f32 v106, v84, v85\n\t" \
    "v_exp_f32 v88, v88\n\t" \
    "v_mfma_f32_32x32x16_bf16 v[0:15], v[154:157], v[96:99], v[0:15]\n\t" \
    "v_exp_f32 v89, v89\n\t" \
    "v_cvt_pk_bf16_f32 v107, v86, v87\n\t" \
    "v_exp_f32 v90, v90\n\t" \
    "v_exp_f32 v91, v91\n\t" \
    "v_cvt_pk_bf16_f32 v108, v88, v89\n\t" \
    "v_exp_f32 v92, v92\n\t" \
    "v_mfma_f32_32x32x16_bf16 v[0:15], v[158:161], v[100:103], v[0:15]\n\t" \
    "v_exp_f32 v93, v93\n\t" \
    "v_cvt_pk_bf16_f32 v109, v90, v91\n\t" \
    "v_exp_f32 v94, v94\n\t" \
    "v_exp_f32 v95, v95\n\t" \
    "v_cvt_pk_bf16_f32 v110, v92, v93\n\t" \
    "v_cvt_pk_bf16_f32 v111, v94, v95\n\t" \
    "v_mfma_f32_32x32x16_bf16 v[80:95], v[150:153], v[124:127], 0\n\t" \
    "v_exp_f32 v64, v64\n\t" \
    "v_exp_f32 v65, v65\n\t" \
    "v_exp_f32 v66, v66\n\t" \
    "v_exp_f32 v67, v67\n\t" \
    "v_cvt_pk_bf16_f32 v96, v64, v65\n\t" \
    "v_exp_f32 v68, v68\n\t" \
    "v_mfma_f32_32x32x8_bf16 v[80:95], v[162:163], v[134:135], v[80:95]\n\t" \
    "v_exp_f32 v69, v69\n\t" \
    "v_cvt_pk_bf16_f32 v97, v66, v67\n\t" \
    "v_exp_f32 v70, v70\n\t" \
    "v_exp_f32 v71, v71\n\t" \
    "v_cvt_pk_bf16_f32 v98, v68, v69\n\t" \
    "v_exp_f32 v72, v72\n\t" \
    "v_mfma_f32_32x32x16_bf16 v[16:31], v[154:157], v[104:107], v[16:31]\n\t" \
    "v_exp_f32 v73, v73\n\t" \
    "v_cvt_pk_bf16_f32 v99, v70, v71\n\t" \
    "v_exp_f32 v74, v74\n\t" \
    "v_exp_f32 v75, v75\n\t" \
    "v_cvt_pk_bf16_f32 v100, v72, v73\n\t" \
    "v_exp_f32 v76, v76\n\t" \
    "v_mfma_f32_32x32x16_bf16 v[16:31], v[158:161], v[108:111], v[16:31]\n\t" \
    "v_exp_f32 v77, v77\n\t" \
    "v_cvt_pk_bf16_f32 v101, v74, v75\n\t" \
    "v_exp_f32 v78, v78\n\t" \
    "v_exp_f32 v79, v79\n\t" \
    "v_cvt_pk_bf16_f32 v102, v76, v77\n\t" \
    "v_cvt_pk_bf16_f32 v103, v78, v79\n\t" \
    "s_waitcnt lgkmcnt(0)\n\t" \
    "v_mfma_f32_32x32x16_bf16 v[64:79], v[136:139], v[112:115], 0\n\t" \
    "v_exp_f32 v80, v80\n\t" \
    "v_exp_f32 v81, v81\n\t" \
    "v_exp_f32 v82, v82\n\t" \
    "v_exp_f32 v83, v83\n\t" \
    "v_cvt_pk_bf16_f32 v104, v80, v81\n\t" \
    "v_exp_f32 v84, v84\n\t" \
    "v_mfma_f32_32x32x8_bf16 v[64:79], v[148:149], v[128:129], v[64:79]\n\t" \
    "v_exp_f32 v85, v85\n\t" \
    "v_cvt_pk_bf16_f32 v105, v82, v83\n\t" \
    "v_exp_f32 v86, v86\n\t" \
    "v_exp_f32 v87, v87\n\t" \
    "v_cvt_pk_bf16_f32 v106, v84, v85\n\t" \
    "v_exp_f32 v88, v88\n\t" \
    "v_mfma_f32_32x32x16_bf16 v[32:47], v[154:157], v[96:99], v[32:47]\n\t" \
    "v_exp_f32 v89, v89\n\t" \
    "v_cvt_pk_bf16_f32 v107, v86, v87\n\t" \
    "v_exp_f32 v90, v90\n\t" \
    "v_exp_f32 v91, v91\n\t" \
    "v_cvt_pk_bf16_f32 v108, v88, v89\n\t" \
    "v_exp_f32 v92, v92\n\t" \
    "v_mfma_f32_32x32x16_bf16 v[32:47], v[158:161], v[100:103], v[32:47]\n\t" \
    "v_exp_f32 v93, v93\n\t" \
    "v_cvt_pk_bf16_f32 v109, v90, v91\n\t" \
    "v_exp_f32 v94, v94\n\t" \
    "v_exp_f32 v95, v95\n\t" \
    "v_cvt_pk_bf16_f32 v110, v92, v93\n\t" \
    "v_cvt_pk_bf16_f32 v111, v94, v95\n\t" \
    "v_mfma_f32_32x32x16_bf16 v[80:95], v[136:139], v[116:119], 0\n\t" \
    "v_exp_f32 v64, v64\n\t" \
    "v_exp_f32 v65, v65\n\t" \
    "v_exp_f32 v66, v66\n\t" \
    "v_exp_f32 v67, v67\n\t" \
    "v_cvt_pk_bf16_f32 v96, v64, v65\n\t" \
    "v_exp_f32 v68, v68\n\t" \
    "v_mfma_f32_32x32x8_bf16 v[80:95], v[148:149], v[130:131], v[80:95]\n\t" \
    "v_exp_f32 v69, v69\n\t" \
    "v_cvt_pk_bf16_f32 v97, v66, v67\n\t" \
    "v_exp_f32 v70, v70\n\t" \
    "v_exp_f32 v71, v71\n\t" \
    "v_cvt_pk_bf16_f32 v98, v68, v69\n\t" \
    "v_exp_f32 v72, v72\n\t" \
    "v_mfma_f32_32x32x16_bf16 v[48:63], v[154:157], v[104:107], v[48:63]\n\t" \
    "v_exp_f32 v73, v73\n\t" \
    "v_cvt_pk_bf16_f32 v99, v70, v71\n\t" \
    "v_exp_f32 v74, v74\n\t" \
    "v_exp_f32 v75, v75\n\t" \
    "v_cvt_pk_bf16_f32 v100, v72, v73\n\t" \
    "v_exp_f32 v76, v76\n\t" \
    "v_mfma_f32_32x32x16_bf16 v[48:63], v[158:161], v[108:111], v[48:63]\n\t" \
    "v_exp_f32 v77, v77\n\t" \
    "v_cvt_pk_bf16_f32 v101, v74, v75\n\t" \
    "v_exp_f32 v78, v78\n\t" \
    "v_exp_f32 v79, v79\n\t" \
    "v_cvt_pk_bf16_f32 v102, v76, v77\n\t" \
    "v_cvt_pk_bf16_f32 v103, v78, v79\n\t" \
    "v_mfma_f32_32x32x16_bf16 v[64:79], v[136:139], v[120:123], 0\n\t" \
    "ds_read_b128 v[150:153], v166 offset:9216\n\t" \
    "ds_read_b64 v[162:163], v167 offset:9216\n\t" \
    "ds_read_b128 v[154:157], v168 offset:9216\n\t" \
    "ds_read_b128 v[158:161], v169 offset:9216\n\t" \
    "ds_read_b32 v172, v170\n\t" \
    "v_exp_f32 v80, v80\n\t" \
    "v_exp_f32 v81, v81\n\t" \
    "v_exp_f32 v82, v82\n\t" \
    "v_exp_f32 v83, v83\n\t" \
    "v_cvt_pk_bf16_f32 v104, v80, v81\n\t" \
    "v_exp_f32 v84, v84\n\t" \
    "v_mfma_f32_32x32x8_bf16 v[64:79], v[148:149], v[132:133], v[64:79]\n\t" \
    "v_exp_f32 v85, v85\n\t" \
    "v_cvt_pk_bf16_f32 v105, v82, v83\n\t" \
    "v_exp_f32 v86, v86\n\t" \
    "v_exp_f32 v87, v87\n\t" \
    "v_cvt_pk_bf16_f32 v106, v84, v85\n\t" \
    "v_exp_f32 v88, v88\n\t" \
    "v_mfma_f32_32x32x16_bf16 v[0:15], v[140:143], v[96:99], v[0:15]\n\t" \
    "v_exp_f32 v89, v89\n\t" \
    "v_cvt_pk_bf16_f32 v107, v86, v87\n\t" \
    "v_exp_f32 v90, v90\n\t" \
    "v_exp_f32 v91, v91\n\t" \
    "v_cvt_pk_bf16_f32 v108, v88, v89\n\t" \
    "v_exp_f32 v92, v92\n\t" \
    "v_mfma_f32_32x32x16_bf16 v[0:15], v[144:147], v[100:103], v[0:15]\n\t" \
    "v_exp_f32 v93, v93\n\t" \
    "v_cvt_pk_bf16_f32 v109, v90, v91\n\t" \
    "v_exp_f32 v94, v94\n\t" \
    "v_exp_f32 v95, v95\n\t" \
    "v_cvt_pk_bf16_f32 v110, v92, v93\n\t" \
    "v_cvt_pk_bf16_f32 v111, v94, v95\n\t" \
    "v_mfma_f32_32x32x16_bf16 v[80:95], v[136:139], v[124:127], 0\n\t" \
    "v_exp_f32 v64, v64\n\t" \
    "v_exp_f32 v65, v65\n\t" \
    "v_exp_f32 v66, v66\n\t" \
    "v_exp_f32 v67, v67\n\t" \
    "v_cvt_pk_bf16_f32 v96, v64, v65\n\t" \
    "v_exp_f32 v68, v68\n\t" \
    "v_mfma_f32_32x32x8_bf16 v[80:95], v[148:149], v[134:135], v[80:95]\n\t" \
    "v_exp_f32 v69, v69\n\t" \
    "v_cvt_pk_bf16_f32 v97, v66, v67\n\t" \
    "v_exp_f32 v70, v70\n\t" \
    "v_exp_f32 v71, v71\n\t" \
    "v_cvt_pk_bf16_f32 v98, v68, v69\n\t" \
    "v_exp_f32 v72, v72\n\t" \
    "v_mfma_f32_32x32x16_bf16 v[16:31], v[140:143], v[104:107], v[16:31]\n\t" \
    "v_exp_f32 v73, v73\n\t" \
    "v_cvt_pk_bf16_f32 v99, v70, v71\n\t" \
    "v_exp_f32 v74, v74\n\t" \
    "v_exp_f32 v75, v75\n\t" \
    "v_cvt_pk_bf16_f32 v100, v72, v73\n\t" \
    "v_exp_f32 v76, v76\n\t" \
    "v_mfma_f32_32x32x16_bf16 v[16:31], v[144:147], v[108:111], v[16:31]\n\t" \
    "v_exp_f32 v77, v77\n\t" \
    "v_cvt_pk_bf16_f32 v101, v74, v75\n\t" \
    "v_exp_f32 v78, v78\n\t" \
    "v_exp_f32 v79, v79\n\t" \
    "v_cvt_pk_bf16_f32 v102, v76, v77\n\t" \
    "v_cvt_pk_bf16_f32 v103, v78, v79\n\t" \
    "s_waitcnt lgkmcnt(0)\n\t" \
    "v_mfma_f32_32x32x16_bf16 v[64:79], v[150:153], v[112:115], 0\n\t" \
    "ds_write_b32 v171, v173\n\t" \
    "v_add_u32 v173, 2, v173\n\t" \
    "v_exp_f32 v80, v80\n\t" \
    "v_exp_f32 v81, v81\n\t" \
    "v_exp_f32 v82, v82\n\t" \
    "v_exp_f32 v83, v83\n\t" \
    "v_cvt_pk_bf16_f32 v104, v80, v81\n\t" \
    "v_exp_f32 v84, v84\n\t" \
    "v_mfma_f32_32x32x8_bf16 v[64:79], v[162:163], v[128:129], v[64:79]\n\t" \
    "v_exp_f32 v85, v85\n\t" \
    "v_cvt_pk_bf16_f32 v105, v82, v83\n\t" \
    "v_exp_f32 v86, v86\n\t" \
    "v_exp_f32 v87, v87\n\t" \
    "v_cvt_pk_bf16_f32 v106, v84, v85\n\t" \
    "v_exp_f32 v88, v88\n\t" \
    "v_mfma_f32_32x32x16_bf16 v[32:47], v[140:143], v[96:99], v[32:47]\n\t" \
    "v_exp_f32 v89, v89\n\t" \
    "v_cvt_pk_bf16_f32 v107, v86, v87\n\t" \
    "v_exp_f32 v90, v90\n\t" \
    "v_exp_f32 v91, v91\n\t" \
    "v_cvt_pk_bf16_f32 v108, v88, v89\n\t" \
    "v_exp_f32 v92, v92\n\t" \
    "v_mfma_f32_32x32x16_bf16 v[32:47], v[144:147], v[100:103], v[32:47]\n\t" \
    "v_exp_f32 v93, v93\n\t" \
    "v_cvt_pk_bf16_f32 v109, v90, v91\n\t" \
    "v_exp_f32 v94, v94\n\t" \
    "v_exp_f32 v95, v95\n\t" \
    "v_cvt_pk_bf16_f32 v110, v92, v93\n\t" \
    "v_cvt_pk_bf16_f32 v111, v94, v95\n\t" \
    "v_mfma_f32_32x32x16_bf16 v[80:95], v[150:153], v[116:119], 0\n\t" \
    "v_exp_f32 v64, v64\n\t" \
    "v_exp_f32 v65, v65\n\t" \
    "v_exp_f32 v66, v66\n\t" \
    "v_exp_f32 v67, v67\n\t" \
    "v_cvt_pk_bf16_f32 v96, v64, v65\n\t" \
    "v_exp_f32 v68, v68\n\t" \
    "v_mfma_f32_32x32x8_bf16 v[80:95], v[162:163], v[130:131], v[80:95]\n\t" \
    "v_exp_f32 v69, v69\n\t" \
    "v_cvt_pk_bf16_f32 v97, v66, v67\n\t" \
    "v_exp_f32 v70, v70\n\t" \
    "v_exp_f32 v71, v71\n\t" \
    "v_cvt_pk_bf16_f32 v98, v68, v69\n\t" \
    "v_exp_f32 v72, v72\n\t" \
    "v_mfma_f32_32x32x16_bf16 v[48:63], v[140:143], v[104:107], v[48:63]\n\t" \
    "v_exp_f32 v73, v73\n\t" \
    "v_cvt_pk_bf16_f32 v99, v70, v71\n\t" \
    "v_exp_f32 v74, v74\n\t" \
    "v_exp_f32 v75, v75\n\t" \
    "v_cvt_pk_bf16_f32 v100, v72, v73\n\t" \
    "v_cmp_le_u32 vcc, v173, v172\n\t" \
    "v_exp_f32 v76, v76\n\t" \
    "v_mfma_f32_32x32x16_bf16 v[48:63], v[144:147], v[108:111], v[48:63]\n\t" \
    "v_exp_f32 v77, v77\n\t" \
    "v_cvt_pk_bf16_f32 v101, v74, v75\n\t" \
    "v_exp_f32 v78, v78\n\t" \
    "v_exp_f32 v79, v79\n\t" \
    "v_cvt_pk_bf16_f32 v102, v76, v77\n\t" \
    "v_cvt_pk_bf16_f32 v103, v78, v79\n\t" \
    "v_mfma_f32_32x32x16_bf16 v[64:79], v[150:153], v[120:123], 0\n\t" \
    "s_cbranch_vccz 12f\n\t" \
    "13:\n\t" \
    "ds_read_b128 v[136:139], v166 offset:12288\n\t" \
    "ds_read_b64 v[148:149], v167 offset:12288\n\t" \
    "ds_read_b128 v[140:143], v168 offset:12288\n\t" \
    "ds_read_b128 v[144:147], v169 offset:12288\n\t" \
    "v_exp_f32 v80, v80\n\t" \
    "v_exp_f32 v81, v81\n\t" \
    "v_exp_f32 v82, v82\n\t" \
    "v_exp_f32 v83, v83\n\t" \
    "v_cvt_pk_bf16_f32 v104, v80, v81\n\t" \
    "v_exp_f32 v84, v84\n\t" \
    "v_mfma_f32_32x32x8_bf16 v[64:79], v[162:163], v[132:133], v[64:79]\n\t" \
    "v_exp_f32 v85, v85\n\t" \
    "v_cvt_pk_bf16_f32 v105, v82, v83\n\t" \
    "v_exp_f32 v86, v86\n\t" \
    "v_exp_f32 v87, v87\n\t" \
    "v_cvt_pk_bf16_f32 v106, v84, v85\n\t" \
    "v_exp_f32 v88, v88\n\t" \
    "v_mfma_f32_32x32x16_bf16 v[0:15], v[154:157], v[96:99], v[0:15]\n\t" \
    "v_exp_f32 v89, v89\n\t" \
    "v_cvt_pk_bf16_f32 v107, v86, v87\n\t" \
    "v_exp_f32 v90, v90\n\t" \
    "v_exp_f32 v91, v91\n\t" \
    "v_cvt_pk_bf16_f32 v108, v88, v89\n\t" \
    "v_exp_f32 v92, v92\n\t" \
    "v_mfma_f32_32x32x16_bf16 v[0:15], v[158:161], v[100:103], v[0:15]\n\t" \
    "v_exp_f32 v93, v93\n\t" \
    "v_cvt_pk_bf16_f32 v109, v90, v91\n\t" \
    "v_exp_f32 v94, v94\n\t" \
    "v_exp_f32 v95, v95\n\t" \
    "v_cvt_pk_bf16_f32 v110, v92, v93\n\t" \
    "v_cvt_pk_bf16_f32 v111, v94, v95\n\t" \
    "v_mfma_f32_32x32x16_bf16 v[80:95], v[150:153], v[124:127], 0\n\t" \
    "v_exp_f32 v64, v64\n\t" \
    "v_exp_f32 v65, v65\n\t" \
    "v_exp_f32 v66, v66\n\t" \
    "v_exp_f32 v67, v67\n\t" \
    "v_cvt_pk_bf16_f32 v96, v64, v65\n\t" \
    "v_exp_f32 v68, v68\n\t" \
    "v_mfma_f32_32x32x8_bf16 v[80:95], v[162:163], v[134:135], v[80:95]\n\t" \
    "v_exp_f32 v69, v69\n\t" \
    "v_cvt_pk_bf16_f32 v97, v66, v67\n\t" \
    "v_exp_f32 v70, v70\n\t" \
    "v_exp_f32 v71, v71\n\t" \
    "v_cvt_pk_bf16_f32 v98, v68, v69\n\t" \
    "v_exp_f32 v72, v72\n\t" \
    "v_mfma_f32_32x32x16_bf16 v[16:31], v[154:157], v[104:107], v[16:31]\n\t" \
    "v_exp_f32 v73, v73\n\t" \
    "v_cvt_pk_bf16_f32 v99, v70, v71\n\t" \
    "v_exp_f32 v74, v74\n\t" \
    "v_exp_f32 v75, v75\n\t" \
    "v_cvt_pk_bf16_f32 v100, v72, v73\n\t" \
    "v_exp_f32 v76, v76\n\t" \
    "v_mfma_f32_32x32x16_bf16 v[16:31], v[158:161], v[108:111], v[16:31]\n\t" \
    "v_exp_f32 v77, v77\n\t" \
    "v_cvt_pk_bf16_f32 v101, v74, v75\n\t" \
    "v_exp_f32 v78, v78\n\t" \
    "v_exp_f32 v79, v79\n\t" \
    "v_cvt_pk_bf16_f32 v102, v76, v77\n\t" \
    "v_cvt_pk_bf16_f32 v103, v78, v79\n\t" \
    "s_waitcnt lgkmcnt(0)\n\t" \
    "v_mfma_f32_32x32x16_bf16 v[64:79], v[136:139], v[112:115], 0\n\t" \
    "v_exp_f32 v80, v80\n\t" \
    "v_exp_f32 v81, v81\n\t" \
    "v_exp_f32 v82, v82\n\t" \
    "v_exp_f32 v83, v83\n\t" \
    "v_cvt_pk_bf16_f32 v104, v80, v81\n\t" \
    "v_exp_f32 v84, v84\n\t" \
    "v_mfma_f32_32x32x8_bf16 v[64:79], v[148:149], v[128:129], v[64:79]\n\t" \
    "v_exp_f32 v85, v85\n\t" \
    "v_cvt_pk_bf16_f32 v105, v82, v83\n\t" \
    "v_exp_f32 v86, v86\n\t" \
    "v_exp_f32 v87, v87\n\t" \
    "v_cvt_pk_bf16_f32 v106, v84, v85\n\t" \
    "v_exp_f32 v88, v88\n\t" \
    "v_mfma_f32_32x32x16_bf16 v[32:47], v[154:157], v[96:99], v[32:47]\n\t" \
    "v_exp_f32 v89, v89\n\t" \
    "v_cvt_pk_bf16_f32 v107, v86, v87\n\t" \
    "v_exp_f32 v90, v90\n\t" \
    "v_exp_f32 v91, v91\n\t" \
    "v_cvt_pk_bf16_f32 v108, v88, v89\n\t" \
    "v_exp_f32 v92, v92\n\t" \
    "v_mfma_f32_32x32x16_bf16 v[32:47], v[158:161], v[100:103], v[32:47]\n\t" \
    "v_exp_f32 v93, v93\n\t" \
    "v_cvt_pk_bf16_f32 v109, v90, v91\n\t" \
    "v_exp_f32 v94, v94\n\t" \
    "v_exp_f32 v95, v95\n\t" \
    "v_cvt_pk_bf16_f32 v110, v92, v93\n\t" \
    "v_cvt_pk_bf16_f32 v111, v94, v95\n\t" \
    "v_mfma_f32_32x32x16_bf16 v[80:95], v[136:139], v[116:119], 0\n\t" \
    "v_exp_f32 v64, v64\n\t" \
    "v_exp_f32 v65, v65\n\t" \
    "v_exp_f32 v66, v66\n\t" \
    "v_exp_f32 v67, v67\n\t" \
    "v_cvt_pk_bf16_f32 v96, v64, v65\n\t" \
    "v_exp_f32 v68, v68\n\t" \
    "v_mfma_f32_32x32x8_bf16 v[80:95], v[148:149], v[130:131], v[80:95]\n\t" \
    "v_exp_f32 v69, v69\n\t" \
    "v_cvt_pk_bf16_f32 v97, v66, v67\n\t" \
    "v_exp_f32 v70, v70\n\t" \
    "v_exp_f32 v71, v71\n\t" \
    "v_cvt_pk_bf16_f32 v98, v68, v69\n\t" \
    "v_exp_f32 v72, v72\n\t" \
    "v_mfma_f32_32x32x16_bf16 v[48:63], v[154:157], v[104:107], v[48:63]\n\t" \
    "v_exp_f32 v73, v73\n\t" \
    "v_cvt_pk_bf16_f32 v99, v70, v71\n\t" \
    "v_exp_f32 v74, v74\n\t" \
    "v_exp_f32 v75, v75\n\t" \
    "v_cvt_pk_bf16_f32 v100, v72, v73\n\t" \
    "v_exp_f32 v76, v76\n\t" \
    "v_mfma_f32_32x32x16_bf16 v[48:63], v[158:161], v[108:111], v[48:63]\n\t" \
    "v_exp_f32 v77, v77\n\t" \
    "v_cvt_pk_bf16_f32 v101, v74, v75\n\t" \
    "v_exp_f32 v78, v78\n\t" \
    "v_exp_f32 v79, v79\n\t" \
    "v_cvt_pk_bf16_f32 v102, v76, v77\n\t" \
    "v_cvt_pk_bf16_f32 v103, v78, v79\n\t" \
    "v_mfma_f32_32x32x16_bf16 v[64:79], v[136:139], v[120:123], 0\n\t" \
    "ds_read_b128 v[150:153], v166 offset:15360\n\t" \
    "ds_read_b64 v[162:163], v167 offset:15360\n\t" \
    "ds_read_b128 v[154:157], v168 offset:15360\n\t" \
    "ds_read_b128 v[158:161], v169 offset:15360\n\t" \
    "ds_read_b32 v172, v170\n\t" \
    "v_exp_f32 v80, v80\n\t" \
    "v_exp_f32 v81, v81\n\t" \
    "v_exp_f32 v82, v82\n\t" \
    "v_exp_f32 v83, v83\n\t" \
    "v_cvt_pk_bf16_f32 v104, v80, v81\n\t" \
    "v_exp_f32 v84, v84\n\t" \
    "v_mfma_f32_32x32x8_bf16 v[64:79], v[148:149], v[132:133], v[64:79]\n\t" \
    "v_exp_f32 v85, v85\n\t" \
    "v_cvt_pk_bf16_f32 v105, v82, v83\n\t" \
    "v_exp_f32 v86, v86\n\t" \
    "v_exp_f32 v87, v87\n\t" \
    "v_cvt_pk_bf16_f32 v106, v84, v85\n\t" \
    "v_exp_f32 v88, v88\n\t" \
    "v_mfma_f32_32x32x16_bf16 v[0:15], v[140:143], v[96:99], v[0:15]\n\t" \
    "v_exp_f32 v89, v89\n\t" \
    "v_cvt_pk_bf16_f32 v107, v86, v87\n\t" \
    "v_exp_f32 v90, v90\n\t" \
    "v_exp_f32 v91, v91\n\t" \
    "v_cvt_pk_bf16_f32 v108, v88, v89\n\t" \
    "v_exp_f32 v92, v92\n\t" \
    "v_mfma_f32_32x32x16_bf16 v[0:15], v[144:147], v[100:103], v[0:15]\n\t" \
    "v_exp_f32 v93, v93\n\t" \
    "v_cvt_pk_bf16_f32 v109, v90, v91\n\t" \
    "v_exp_f32 v94, v94\n\t" \
    "v_exp_f32 v95, v95\n\t" \
    "v_cvt_pk_bf16_f32 v110, v92, v93\n\t" \
    "v_cvt_pk_bf16_f32 v111, v94, v95\n\t" \
    "v_mfma_f32_32x32x16_bf16 v[80:95], v[136:139], v[124:127], 0\n\t" \
    "v_exp_f32 v64, v64\n\t" \
    "v_exp_f32 v65, v65\n\t" \
    "v_exp_f32 v66, v66\n\t" \
    "v_exp_f32 v67, v67\n\t" \
    "v_cvt_pk_bf16_f32 v96, v64, v65\n\t" \
    "v_exp_f32 v68, v68\n\t" \
    "v_mfma_f32_32x32x8_bf16 v[80:95], v[148:149], v[134:135], v[80:95]\n\t" \
    "v_exp_f32 v69, v69\n\t" \
    "v_cvt_pk_bf16_f32 v97, v66, v67\n\t" \
    "v_exp_f32 v70, v70\n\t" \
    "v_exp_f32 v71, v71\n\t" \
    "v_cvt_pk_bf16_f32 v98, v68, v69\n\t" \
    "v_exp_f32 v72, v72\n\t" \
    "v_mfma_f32_32x32x16_bf16 v[16:31], v[140:143], v[104:107], v[16:31]\n\t" \
    "v_exp_f32 v73, v73\n\t" \
    "v_cvt_pk_bf16_f32 v99, v70, v71\n\t" \
    "v_exp_f32 v74, v74\n\t" \
    "v_exp_f32 v75, v75\n\t" \
    "v_cvt_pk_bf16_f32 v100, v72, v73\n\t" \
    "v_exp_f32 v76, v76\n\t" \
    "v_mfma_f32_32x32x16_bf16 v[16:31], v[144:147], v[108:111], v[16:31]\n\t" \
    "v_exp_f32 v77, v77\n\t" \
    "v_cvt_pk_bf16_f32 v101, v74, v75\n\t" \
    "v_exp_f32 v78, v78\n\t" \
    "v_exp_f32 v79, v79\n\t" \
    "v_cvt_pk_bf16_f32 v102, v76, v77\n\t" \
    "v_cvt_pk_bf16_f32 v103, v78, v79\n\t" \
    "s_waitcnt lgkmcnt(0)\n\t" \
    "v_mfma_f32_32x32x16_bf16 v[64:79], v[150:153], v[112:115], 0\n\t" \
    "ds_write_b32 v171, v173\n\t" \
    "v_add_u32 v173, 2, v173\n\t" \
    "v_exp_f32 v80, v80\n\t" \
    "v_exp_f32 v81, v81\n\t" \
    "v_exp_f32 v82, v82\n\t" \
    "v_exp_f32 v83, v83\n\t" \
    "v_cvt_pk_bf16_f32 v104, v80, v81\n\t" \
    "v_exp_f32 v84, v84\n\t" \
    "v_mfma_f32_32x32x8_bf16 v[64:79], v[162:163], v[128:129], v[64:79]\n\t" \
    "v_exp_f32 v85, v85\n\t" \
    "v_cvt_pk_bf16_f32 v105, v82, v83\n\t" \
    "v_exp_f32 v86, v86\n\t" \
    "v_exp_f32 v87, v87\n\t" \
    "v_cvt_pk_bf16_f32 v106, v84, v85\n\t" \
    "v_exp_f32 v88, v88\n\t" \
    "v_mfma_f32_32x32x16_bf16 v[32:47], v[140:143], v[96:99], v[32:47]\n\t" \
    "v_exp_f32 v89, v89\n\t" \
    "v_cvt_pk_bf16_f32 v107, v86, v87\n\t" \
    "v_exp_f32 v90, v90\n\t" \
    "v_exp_f32 v91, v91\n\t" \
    "v_cvt_pk_bf16_f32 v108, v88, v89\n\t" \
    "v_exp_f32 v92, v92\n\t" \
    "v_mfma_f32_32x32x16_bf16 v[32:47], v[144:147], v[100:103], v[32:47]\n\t" \
    "v_exp_f32 v93, v93\n\t" \
    "v_cvt_pk_bf16_f32 v109, v90, v91\n\t" \
    "v_exp_f32 v94, v94\n\t" \
    "v_exp_f32 v95, v95\n\t" \
    "v_cvt_pk_bf16_f32 v110, v92, v93\n\t" \
    "v_cvt_pk_bf16_f32 v111, v94, v95\n\t" \
    "v_mfma_f32_32x32x16_bf16 v[80:95], v[150:153], v[116:119], 0\n\t" \
    "v_exp_f32 v64, v64\n\t" \
    "v_exp_f32 v65, v65\n\t" \
    "v_exp_f32 v66, v66\n\t" \
    "v_exp_f32 v67, v67\n\t" \
    "v_cvt_pk_bf16_f32 v96, v64, v65\n\t" \
    "v_exp_f32 v68, v68\n\t" \
    "v_mfma_f32_32x32x8_bf16 v[80:95], v[162:163], v[130:131], v[80:95]\n\t" \
    "v_exp_f32 v69, v69\n\t" \
    "v_cvt_pk_bf16_f32 v97, v66, v67\n\t" \
    "v_exp_f32 v70, v70\n\t" \
    "v_exp_f32 v71, v71\n\t" \
    "v_cvt_pk_bf16_f32 v98, v68, v69\n\t" \
    "v_exp_f32 v72, v72\n\t" \
    "v_mfma_f32_32x32x16_bf16 v[48:63], v[140:143], v[104:107], v[48:63]\n\t" \
    "v_exp_f32 v73, v73\n\t" \
    "v_cvt_pk_bf16_f32 v99, v70, v71\n\t" \
    "v_exp_f32 v74, v74\n\t" \
    "v_exp_f32 v75, v75\n\t" \
    "v_cvt_pk_bf16_f32 v100, v72, v73\n\t" \
    "v_cmp_le_u32 vcc, v173, v172\n\t" \
    "v_exp_f32 v76, v76\n\t" \
    "v_mfma_f32_32x32x16_bf16 v[48:63], v[144:147], v[108:111], v[48:63]\n\t" \
    "v_exp_f32 v77, v77\n\t" \
    "v_cvt_pk_bf16_f32 v101, v74, v75\n\t" \
    "v_exp_f32 v78, v78\n\t" \
    "v_exp_f32 v79, v79\n\t" \
    "v_cvt_pk_bf16_f32 v102, v76, v77\n\t" \
    "v_cvt_pk_bf16_f32 v103, v78, v79\n\t" \
    "v_mfma_f32_32x32x16_bf16 v[64:79], v[150:153], v[120:123], 0\n\t" \
    "s_cbranch_vccz 14f\n\t" \
    "15:\n\t" \
    "ds_read_b128 v[136:139], v166 offset:18432\n\t" \
    "ds_read_b64 v[148:149], v167 offset:18432\n\t" \
    "ds_read_b128 v[140:143], v168 offset:18432\n\t" \
    "ds_read_b128 v[144:147], v169 offset:18432\n\t" \
    "v_exp_f32 v80, v80\n\t" \
    "v_exp_f32 v81, v81\n\t" \
    "v_exp_f32 v82, v82\n\t" \
    "v_exp_f32 v83, v83\n\t" \
    "v_cvt_pk_bf16_f32 v104, v80, v81\n\t" \
    "v_exp_f32 v84, v84\n\t" \
    "v_mfma_f32_32x32x8_bf16 v[64:79], v[162:163], v[132:133], v[64:79]\n\t" \
    "v_exp_f32 v85, v85\n\t" \
    "v_cvt_pk_bf16_f32 v105, v82, v83\n\t" \
    "v_exp_f32 v86, v86\n\t" \
    "v_exp_f32 v87, v87\n\t" \
    "v_cvt_pk_bf16_f32 v106, v84, v85\n\t" \
    "v_exp_f32 v88, v88\n\t" \
    "v_mfma_f32_32x32x16_bf16 v[0:15], v[154:157], v[96:99], v[0:15]\n\t" \
    "v_exp_f32 v89, v89\n\t" \
    "v_cvt_pk_bf16_f32 v107, v86, v87\n\t" \
    "v_exp_f32 v90, v90\n\t" \
    "v_exp_f32 v91, v91\n\t" \
    "v_cvt_pk_bf16_f32 v108, v88, v89\n\t" \
    "v_exp_f32 v92, v92\n\t" \
    "v_mfma_f32_32x32x16_bf16 v[0:15], v[158:161], v[100:103], v[0:15]\n\t" \
    "v_exp_f32 v93, v93\n\t" \
    "v_cvt_pk_bf16_f32 v109, v90, v91\n\t" \
    "v_exp_f32 v94, v94\n\t" \
    "v_exp_f32 v95, v95\n\t" \
    "v_cvt_pk_bf16_f32 v110, v92, v93\n\t" \
    "v_cvt_pk_bf16_f32 v111, v94, v95\n\t" \
    "v_mfma_f32_32x32x16_bf16 v[80:95], v[150:153], v[124:127], 0\n\t" \
    "v_exp_f32 v64, v64\n\t" \
    "v_exp_f32 v65, v65\n\t" \
    "v_exp_f32 v66, v66\n\t" \
    "v_exp_f32 v67, v67\n\t" \
    "v_cvt_pk_bf16_f32 v96, v64, v65\n\t" \
    "v_exp_f32 v68, v68\n\t" \
    "v_mfma_f32_32x32x8_bf16 v[80:95], v[162:163], v[134:135], v[80:95]\n\t" \
    "v_exp_f32 v69, v69\n\t" \
    "v_cvt_pk_bf16_f32 v97, v66, v67\n\t" \
    "v_exp_f32 v70, v70\n\t" \
    "v_exp_f32 v71, v71\n\t" \
    "v_cvt_pk_bf16_f32 v98, v68, v69\n\t" \
    "v_exp_f32 v72, v72\n\t" \
    "v_mfma_f32_32x32x16_bf16 v[16:31], v[154:157], v[104:107], v[16:31]\n\t" \
    "v_exp_f32 v73, v73\n\t" \
    "v_cvt_pk_bf16_f32 v99, v70, v71\n\t" \
    "v_exp_f32 v74, v74\n\t" \
    "v_exp_f32 v75, v75\n\t" \
    "v_cvt_pk_bf16_f32 v100, v72, v73\n\t" \
    "v_exp_f32 v76, v76\n\t" \
    "v_mfma_f32_32x32x16_bf16 v[16:31], v[158:161], v[108:111], v[16:31]\n\t" \
    "v_exp_f32 v77, v77\n\t" \
    "v_cvt_pk_bf16_f32 v101, v74, v75\n\t" \
    "v_exp_f32 v78, v78\n\t" \
    "v_exp_f32 v79, v79\n\t" \
    "v_cvt_pk_bf16_f32 v102, v76, v77\n\t" \
    "v_cvt_pk_bf16_f32 v103, v78, v79\n\t" \
    "s_waitcnt lgkmcnt(0)\n\t" \
    "v_mfma_f32_32x32x16_bf16 v[64:79], v[136:139], v[112:115], 0\n\t" \
    "v_exp_f32 v80, v80\n\t" \
    "v_exp_f32 v81, v81\n\t" \
    "v_exp_f32 v82, v82\n\t" \
    "v_exp_f32 v83, v83\n\t" \
    "v_cvt_pk_bf16_f32 v104, v80, v81\n\t" \
    "v_exp_f32 v84, v84\n\t" \
    "v_mfma_f32_32x32x8_bf16 v[64:79], v[148:149], v[128:129], v[64:79]\n\t" \
    "v_exp_f32 v85, v85\n\t" \
    "v_cvt_pk_bf16_f32 v105, v82, v83\n\t" \
    "v_exp_f32 v86, v86\n\t" \
    "v_exp_f32 v87, v87\n\t" \
    "v_cvt_pk_bf16_f32 v106, v84, v85\n\t" \
    "v_exp_f32 v88, v88\n\t" \
    "v_mfma_f32_32x32x16_bf16 v[32:47], v[154:157], v[96:99], v[32:47]\n\t" \
    "v_exp_f32 v89, v89\n\t" \
    "v_cvt_pk_bf16_f32 v107, v86, v87\n\t" \
    "v_exp_f32 v90, v90\n\t" \
    "v_exp_f32 v91, v91\n\t" \
    "v_cvt_pk_bf16_f32 v108, v88, v89\n\t" \
    "v_exp_f32 v92, v92\n\t" \
    "v_mfma_f32_32x32x16_bf16 v[32:47], v[158:161], v[100:103], v[32:47]\n\t" \
    "v_exp_f32 v93, v93\n\t" \
    "v_cvt_pk_bf16_f32 v109, v90, v91\n\t" \
    "v_exp_f32 v94, v94\n\t" \
    "v_exp_f32 v95, v95\n\t" \
    "v_cvt_pk_bf16_f32 v110, v92, v93\n\t" \
    "v_cvt_pk_bf16_f32 v111, v94, v95\n\t" \
    "v_mfma_f32_32x32x16_bf16 v[80:95], v[136:139], v[116:119], 0\n\t" \
    "v_exp_f32 v64, v64\n\t" \
    "v_exp_f32 v65, v65\n\t" \
    "v_exp_f32 v66, v66\n\t" \
    "v_exp_f32 v67, v67\n\t" \
    "v_cvt_pk_bf16_f32 v96, v64, v65\n\t" \
    "v_exp_f32 v68, v68\n\t" \
    "v_mfma_f32_32x32x8_bf16 v[80:95], v[148:149], v[130:131], v[80:95]\n\t" \
    "v_exp_f32 v69, v69\n\t" \
    "v_cvt_pk_bf16_f32 v97, v66, v67\n\t" \
    "v_exp_f32 v70, v70\n\t" \
    "v_exp_f32 v71, v71\n\t" \
    "v_cvt_pk_bf16_f32 v98, v68, v69\n\t" \
    "v_exp_f32 v72, v72\n\t" \
    "v_mfma_f32_32x32x16_bf16 v[48:63], v[154:157], v[104:107], v[48:63]\n\t" \
    "v_exp_f32 v73, v73\n\t" \
    "v_cvt_pk_bf16_f32 v99, v70, v71\n\t" \
    "v_exp_f32 v74, v74\n\t" \
    "v_exp_f32 v75, v75\n\t" \
    "v_cvt_pk_bf16_f32 v100, v72, v73\n\t" \
    "v_exp_f32 v76, v76\n\t" \
    "v_mfma_f32_32x32x16_bf16 v[48:63], v[158:161], v[108:111], v[48:63]\n\t" \
    "v_exp_f32 v77, v77\n\t" \
    "v_cvt_pk_bf16_f32 v101, v74, v75\n\t" \
    "v_exp_f32 v78, v78\n\t" \
    "v_exp_f32 v79, v79\n\t" \
    "v_cvt_pk_bf16_f32 v102, v76, v77\n\t" \
    "v_cvt_pk_bf16_f32 v103, v78, v79\n\t" \
    "v_mfma_f32_32x32x16_bf16 v[64:79], v[136:139], v[120:123], 0\n\t" \
    "ds_read_b128 v[150:153], v166 offset:21504\n\t" \
    "ds_read_b64 v[162:163], v167 offset:21504\n\t" \
    "ds_read_b128 v[154:157], v168 offset:21504\n\t" \
    "ds_read_b128 v[158:161], v169 offset:21504\n\t" \
    "ds_read_b32 v172, v170\n\t" \
    "v_exp_f32 v80, v80\n\t" \
    "v_exp_f32 v81, v81\n\t" \
    "v_exp_f32 v82, v82\n\t" \
    "v_exp_f32 v83, v83\n\t" \
    "v_cvt_pk_bf16_f32 v104, v80, v81\n\t" \
    "v_exp_f32 v84, v84\n\t" \
    "v_mfma_f32_32x32x8_bf16 v[64:79], v[148:149], v[132:133], v[64:79]\n\t" \
    "v_exp_f32 v85, v85\n\t" \
    "v_cvt_pk_bf16_f32 v105, v82, v83\n\t" \
    "v_exp_f32 v86, v86\n\t" \
    "v_exp_f32 v87, v87\n\t" \
    "v_cvt_pk_bf16_f32 v106, v84, v85\n\t" \
    "v_exp_f32 v88, v88\n\t" \
    "v_mfma_f32_32x32x16_bf16 v[0:15], v[140:143], v[96:99], v[0:15]\n\t" \
    "v_exp_f32 v89, v89\n\t" \
    "v_cvt_pk_bf16_f32 v107, v86, v87\n\t" \
    "v_exp_f32 v90, v90\n\t" \
    "v_exp_f32 v91, v91\n\t" \
    "v_cvt_pk_bf16_f32 v108, v88, v89\n\t" \
    "v_exp_f32 v92, v92\n\t" \
    "v_mfma_f32_32x32x16_bf16 v[0:15], v[144:147], v[100:103], v[0:15]\n\t" \
    "v_exp_f32 v93, v93\n\t" \
    "v_cvt_pk_bf16_f32 v109, v90, v91\n\t" \
    "v_exp_f32 v94, v94\n\t" \
    "v_exp_f32 v95, v95\n\t" \
    "v_cvt_pk_bf16_f32 v110, v92, v93\n\t" \
    "v_cvt_pk_bf16_f32 v111, v94, v95\n\t" \
    "v_mfma_f32_32x32x16_bf16 v[80:95], v[136:139], v[124:127], 0\n\t" \
    "v_exp_f32 v64, v64\n\t" \
    "v_exp_f32 v65, v65\n\t" \
    "v_exp_f32 v66, v66\n\t" \
    "v_exp_f32 v67, v67\n\t" \
    "v_cvt_pk_bf16_f32 v96, v64, v65\n\t" \
    "v_exp_f32 v68, v68\n\t" \
    "v_mfma_f32_32x32x8_bf16 v[80:95], v[148:149], v[134:135], v[80:95]\n\t" \
    "v_exp_f32 v69, v69\n\t" \
    "v_cvt_pk_bf16_f32 v97, v66, v67\n\t" \
    "v_exp_f32 v70, v70\n\t" \
    "v_exp_f32 v71, v71\n\t" \
    "v_cvt_pk_bf16_f32 v98, v68, v69\n\t" \
    "v_exp_f32 v72, v72\n\t" \
    "v_mfma_f32_32x32x16_bf16 v[16:31], v[140:143], v[104:107], v[16:31]\n\t" \
    "v_exp_f32 v73, v73\n\t" \
    "v_cvt_pk_bf16_f32 v99, v70, v71\n\t" \
    "v_exp_f32 v74, v74\n\t" \
    "v_exp_f32 v75, v75\n\t" \
    "v_cvt_pk_bf16_f32 v100, v72, v73\n\t" \
    "v_exp_f32 v76, v76\n\t" \
    "v_mfma_f32_32x32x16_bf16 v[16:31], v[144:147], v[108:111], v[16:31]\n\t" \
    "v_exp_f32 v77, v77\n\t" \
    "v_cvt_pk_bf16_f32 v101, v74, v75\n\t" \
    "v_exp_f32 v78, v78\n\t" \
    "v_exp_f32 v79, v79\n\t" \
    "v_cvt_pk_bf16_f32 v102, v76, v77\n\t" \
    "v_cvt_pk_bf16_f32 v103, v78, v79\n\t" \
    "s_waitcnt lgkmcnt(0)\n\t" \
    "v_mfma_f32_32x32x16_bf16 v[64:79], v[150:153], v[112:115], 0\n\t" \
    "ds_write_b32 v171, v173\n\t" \
    "v_add_u32 v173, 2, v173\n\t" \
    "v_exp_f32 v80, v80\n\t" \
    "v_exp_f32 v81, v81\n\t" \
    "v_exp_f32 v82, v82\n\t" \
    "v_exp_f32 v83, v83\n\t" \
    "v_cvt_pk_bf16_f32 v104, v80, v81\n\t" \
    "v_exp_f32 v84, v84\n\t" \
    "v_mfma_f32_32x32x8_bf16 v[64:79], v[162:163], v[128:129], v[64:79]\n\t" \
    "v_exp_f32 v85, v85\n\t" \
    "v_cvt_pk_bf16_f32 v105, v82, v83\n\t" \
    "v_exp_f32 v86, v86\n\t" \
    "v_exp_f32 v87, v87\n\t" \
    "v_cvt_pk_bf16_f32 v106, v84, v85\n\t" \
    "v_exp_f32 v88, v88\n\t" \
    "v_mfma_f32_32x32x16_bf16 v[32:47], v[140:143], v[96:99], v[32:47]\n\t" \
    "v_exp_f32 v89, v89\n\t" \
    "v_cvt_pk_bf16_f32 v107, v86, v87\n\t" \
    "v_exp_f32 v90, v90\n\t" \
    "v_exp_f32 v91, v91\n\t" \
    "v_cvt_pk_bf16_f32 v108, v88, v89\n\t" \
    "v_exp_f32 v92, v92\n\t" \
    "v_mfma_f32_32x32x16_bf16 v[32:47], v[144:147], v[100:103], v[32:47]\n\t" \
    "v_exp_f32 v93, v93\n\t" \
    "v_cvt_pk_bf16_f32 v109, v90, v91\n\t" \
    "v_exp_f32 v94, v94\n\t" \
    "v_exp_f32 v95, v95\n\t" \
    "v_cvt_pk_bf16_f32 v110, v92, v93\n\t" \
    "v_cvt_pk_bf16_f32 v111, v94, v95\n\t" \
    "v_mfma_f32_32x32x16_bf16 v[80:95], v[150:153], v[116:119], 0\n\t" \
    "v_exp_f32 v64, v64\n\t" \
    "v_exp_f32 v65, v65\n\t" \
    "v_exp_f32 v66, v66\n\t" \
    "v_exp_f32 v67, v67\n\t" \
    "v_cvt_pk_bf16_f32 v96, v64, v65\n\t" \
    "v_exp_f32 v68, v68\n\t" \
    "v_mfma_f32_32x32x8_bf16 v[80:95], v[162:163], v[130:131], v[80:95]\n\t" \
    "v_exp_f32 v69, v69\n\t" \
    "v_cvt_pk_bf16_f32 v97, v66, v67\n\t" \
    "v_exp_f32 v70, v70\n\t" \
    "v_exp_f32 v71, v71\n\t" \
    "v_cvt_pk_bf16_f32 v98, v68, v69\n\t" \
    "v_exp_f32 v72, v72\n\t" \
    "v_mfma_f32_32x32x16_bf16 v[48:63], v[140:143], v[104:107], v[48:63]\n\t" \
    "v_exp_f32 v73, v73\n\t" \
    "v_cvt_pk_bf16_f32 v99, v70, v71\n\t" \
    "v_exp_f32 v74, v74\n\t" \
    "v_exp_f32 v75, v75\n\t" \
    "v_cvt_pk_bf16_f32 v100, v72, v73\n\t" \
    "v_cmp_le_u32 vcc, v173, v172\n\t" \
    "v_exp_f32 v76, v76\n\t" \
    "v_mfma_f32_32x32x16_bf16 v[48:63], v[144:147], v[108:111], v[48:63]\n\t" \
    "v_exp_f32 v77, v77\n\t" \
    "v_cvt_pk_bf16_f32 v101, v74, v75\n\t" \
    "v_exp_f32 v78, v78\n\t" \
    "v_exp_f32 v79, v79\n\t" \
    "v_cvt_pk_bf16_f32 v102, v76, v77\n\t" \
    "v_cvt_pk_bf16_f32 v103, v78, v79\n\t" \
    "v_mfma_f32_32x32x16_bf16 v[64:79], v[150:153], v[120:123], 0\n\t" \
    "s_cbranch_vccz 16f\n\t" \
    "17:\n\t" \
    "ds_read_b128 v[136:139], v166 offset:0\n\t" \
    "ds_read_b64 v[148:149], v167 offset:0\n\t" \
    "ds_read_b128 v[140:143], v168 offset:0\n\t" \
    "ds_read_b128 v[144:147], v169 offset:0\n\t" \
    "v_exp_f32 v80, v80\n\t" \
    "v_exp_f32 v81, v81\n\t" \
    "v_exp_f32 v82, v82\n\t" \
    "v_exp_f32 v83, v83\n\t" \
    "v_cvt_pk_bf16_f32 v104, v80, v81\n\t" \
    "v_exp_f32 v84, v84\n\t" \
    "v_mfma_f32_32x32x8_bf16 v[64:79], v[162:163], v[132:133], v[64:79]\n\t" \
    "v_exp_f32 v85, v85\n\t" \
    "v_cvt_pk_bf16_f32 v105, v82, v83\n\t" \
    "v_exp_f32 v86, v86\n\t" \
    "v_exp_f32 v87, v87\n\t" \
    "v_cvt_pk_bf16_f32 v106, v84, v85\n\t" \
    "v_exp_f32 v88, v88\n\t" \
    "v_mfma_f32_32x32x16_bf16 v[0:15], v[154:157], v[96:99], v[0:15]\n\t" \
    "v_exp_f32 v89, v89\n\t" \
    "v_cvt_pk_bf16_f32 v107, v86, v87\n\t" \
    "v_exp_f32 v90, v90\n\t" \
    "v_exp_f32 v91, v91\n\t" \
    "v_cvt_pk_bf16_f32 v108, v88, v89\n\t" \
    "v_exp_f32 v92, v92\n\t" \
    "v_mfma_f32_32x32x16_bf16 v[0:15], v[158:161], v[100:103], v[0:15]\n\t" \
    "v_exp_f32 v93, v93\n\t" \
    "v_cvt_pk_bf16_f32 v109, v90, v91\n\t" \
    "v_exp_f32 v94, v94\n\t" \
    "v_exp_f32 v95, v95\n\t" \
    "v_cvt_pk_bf16_f32 v110, v92, v93\n\t" \
    "v_cvt_pk_bf16_f32 v111, v94, v95\n\t" \
    "v_mfma_f32_32x32x16_bf16 v[80:95], v[150:153], v[124:127], 0\n\t" \
    "v_exp_f32 v64, v64\n\t" \
    "v_exp_f32 v65, v65\n\t" \
    "v_exp_f32 v66, v66\n\t" \
    "v_exp_f32 v67, v67\n\t" \
    "v_cvt_pk_bf16_f32 v96, v64, v65\n\t" \
    "v_exp_f32 v68, v68\n\t" \
    "v_mfma_f32_32x32x8_bf16 v[80:95], v[162:163], v[134:135], v[80:95]\n\t" \
    "v_exp_f32 v69, v69\n\t" \
    "v_cvt_pk_bf16_f32 v97, v66, v67\n\t" \
    "v_exp_f32 v70, v70\n\t" \
    "v_exp_f32 v71, v71\n\t" \
    "v_cvt_pk_bf16_f32 v98, v68, v69\n\t" \
    "v_exp_f32 v72, v72\n\t" \
    "v_mfma_f32_32x32x16_bf16 v[16:31], v[154:157], v[104:107], v[16:31]\n\t" \
    "v_exp_f32 v73, v73\n\t" \
    "v_cvt_pk_bf16_f32 v99, v70, v71\n\t" \
    "v_exp_f32 v74, v74\n\t" \
    "v_exp_f32 v75, v75\n\t" \
    "v_cvt_pk_bf16_f32 v100, v72, v73\n\t" \
    "v_exp_f32 v76, v76\n\t" \
    "v_mfma_f32_32x32x16_bf16 v[16:31], v[158:161], v[108:111], v[16:31]\n\t" \
    "v_exp_f32 v77, v77\n\t" \
    "v_cvt_pk_bf16_f32 v101, v74, v75\n\t" \
    "v_exp_f32 v78, v78\n\t" \
    "v_exp_f32 v79, v79\n\t" \
    "v_cvt_pk_bf16_f32 v102, v76, v77\n\t" \
    "v_cvt_pk_bf16_f32 v103, v78, v79\n\t" \
    "s_waitcnt lgkmcnt(0)\n\t" \
    "v_mfma_f32_32x32x16_bf16 v[64:79], v[136:139], v[112:115], 0\n\t" \
    "v_exp_f32 v80, v80\n\t" \
    "v_exp_f32 v81, v81\n\t" \
    "v_exp_f32 v82, v82\n\t" \
    "v_exp_f32 v83, v83\n\t" \
    "v_cvt_pk_bf16_f32 v104, v80, v81\n\t" \
    "v_exp_f32 v84, v84\n\t" \
    "v_mfma_f32_32x32x8_bf16 v[64:79], v[148:149], v[128:129], v[64:79]\n\t" \
    "s_cmp_eq_u32 s48, 1\n\t" \
    "s_cbranch_scc1 5f\n\t" \
    "6:\n\t" \
    "v_exp_f32 v85, v85\n\t" \
    "v_cvt_pk_bf16_f32 v105, v82, v83\n\t" \
    "v_exp_f32 v86, v86\n\t" \
    "v_exp_f32 v87, v87\n\t" \
    "v_cvt_pk_bf16_f32 v106, v84, v85\n\t" \
    "v_exp_f32 v88, v88\n\t" \
    "v_mfma_f32_32x32x16_bf16 v[32:47], v[154:157], v[96:99], v[32:47]\n\t" \
    "v_exp_f32 v89, v89\n\t" \
    "v_cvt_pk_bf16_f32 v107, v86, v87\n\t" \
    "v_exp_f32 v90, v90\n\t" \
    "v_exp_f32 v91, v91\n\t" \
    "v_cvt_pk_bf16_f32 v108, v88, v89\n\t" \
    "v_exp_f32 v92, v92\n\t" \
    "v_mfma_f32_32x32x16_bf16 v[32:47], v[158:161], v[100:103], v[32:47]\n\t" \
    "v_exp_f32 v93, v93\n\t" \
    "v_cvt_pk_bf16_f32 v109, v90, v91\n\t" \
    "v_exp_f32 v94, v94\n\t" \
    "v_exp_f32 v95, v95\n\t" \
    "v_cvt_pk_bf16_f32 v110, v92, v93\n\t" \
    "v_cvt_pk_bf16_f32 v111, v94, v95\n\t" \
    "s_add_i32 s48, s48, -1\n\t" \
    "s_cmp_lg_u32 s48, 0\n\t" \
    "s_cbranch_scc1 1b\n\t" \
    "v_mfma_f32_32x32x16_bf16 v[48:63], v[154:157], v[104:107], v[48:63]\n\t" \
    "v_mfma_f32_32x32x16_bf16 v[48:63], v[158:161], v[108:111], v[48:63]\n\t" \
    "s_waitcnt lgkmcnt(0)\n\t" \
    "s_barrier\n\t" \
    "ds_read_b128 v[0:3], v175 offset:0\n\t" \
    "ds_read_b128 v[4:7], v175 offset:1024\n\t" \
    "ds_read_b128 v[8:11], v175 offset:2048\n\t" \
    "ds_read_b128 v[12:15], v175 offset:3072\n\t" \
    "ds_read_b128 v[16:19], v175 offset:4096\n\t" \
    "ds_read_b128 v[20:23], v175 offset:5120\n\t" \
    "s_waitcnt lgkmcnt(3)\n\t" \
    "v_pk_add_f32 v[40:41], v[40:41], v[8:9]\n\t" \
    "v_mov_b32 v44, v40\n\t" \
    "v_mov_b32 v45, v40\n\t" \
    "v_pk_add_f32 v[32:33], v[32:33], v[0:1]\n\t" \
    "v_pk_add_f32 v[34:35], v[34:35], v[2:3]\n\t" \
    "v_permlane32_swap_b32 v45, v44\n\t" \
    "v_pk_add_f32 v[36:37], v[36:37], v[4:5]\n\t" \
    "v_pk_add_f32 v[38:39], v[38:39], v[6:7]\n\t" \
    "v_rcp_f32 v46, v44\n\t" \
    "v_pk_add_f32 v[42:43], v[42:43], v[10:11]\n\t" \
    "v_pk_mul_f32 v[32:33], v[32:33], v[46:47] op_sel_hi:[1,0]\n\t" \
    "v_pk_mul_f32 v[34:35], v[34:35], v[46:47] op_sel_hi:[1,0]\n\t" \
    "v_pk_mul_f32 v[36:37], v[36:37], v[46:47] op_sel_hi:[1,0]\n\t" \
    "v_pk_mul_f32 v[38:39], v[38:39], v[46:47] op_sel_hi:[1,0]\n\t" \
    "v_pk_mul_f32 v[40:41], v[40:41], v[46:47] op_sel_hi:[1,0]\n\t" \
    "v_pk_mul_f32 v[42:43], v[42:43], v[46:47] op_sel_hi:[1,0]\n\t" \
    "v_cvt_pk_bf16_f32 v64, v32, v33\n\t" \
    "v_cvt_pk_bf16_f32 v65, v34, v35\n\t" \
    "v_cvt_pk_bf16_f32 v66, v36, v37\n\t" \
    "v_cvt_pk_bf16_f32 v67, v38, v39\n\t" \
    "v_cvt_pk_bf16_f32 v68, v40, v41\n\t" \
    "v_cvt_pk_bf16_f32 v69, v42, v43\n\t" \
    "global_store_dwordx4 v240, v[64:67], s[50:51] sc1\n\t" \
    "s_mov_b32 exec_hi, 0\n\t" \
    "global_store_dwordx2 v241, v[68:69], s[50:51] sc1\n\t" \
    "s_mov_b32 exec_hi, -1\n\t" \
    "s_waitcnt lgkmcnt(0)\n\t" \
    "s_nop 3\n\t" \
    "v_pk_add_f32 v[56:57], v[56:57], v[20:21]\n\t" \
    "v_mov_b32 v60, v56\n\t" \
    "v_mov_b32 v61, v56\n\t" \
    "v_pk_add_f32 v[48:49], v[48:49], v[12:13]\n\t" \
    "v_pk_add_f32 v[50:51], v[50:51], v[14:15]\n\t" \
    "v_permlane32_swap_b32 v61, v60\n\t" \
    "v_pk_add_f32 v[52:53], v[52:53], v[16:17]\n\t" \
    "v_pk_add_f32 v[54:55], v[54:55], v[18:19]\n\t" \
    "v_rcp_f32 v62, v60\n\t" \
    "v_pk_add_f32 v[58:59], v[58:59], v[22:23]\n\t" \
    "v_pk_mul_f32 v[48:49], v[48:49], v[62:63] op_sel_hi:[1,0]\n\t" \
    "v_pk_mul_f32 v[50:51], v[50:51], v[62:63] op_sel_hi:[1,0]\n\t" \
    "v_pk_mul_f32 v[52:53], v[52:53], v[62:63] op_sel_hi:[1,0]\n\t" \
    "v_pk_mul_f32 v[54:55], v[54:55], v[62:63] op_sel_hi:[1,0]\n\t" \
    "v_pk_mul_f32 v[56:57], v[56:57], v[62:63] op_sel_hi:[1,0]\n\t" \
    "v_pk_mul_f32 v[58:59], v[58:59], v[62:63] op_sel_hi:[1,0]\n\t" \
    "v_cvt_pk_bf16_f32 v72, v48, v49\n\t" \
    "v_cvt_pk_bf16_f32 v73, v50, v51\n\t" \
    "v_cvt_pk_bf16_f32 v74, v52, v53\n\t" \
    "v_cvt_pk_bf16_f32 v75, v54, v55\n\t" \
    "v_cvt_pk_bf16_f32 v76, v56, v57\n\t" \
    "v_cvt_pk_bf16_f32 v77, v58, v59\n\t" \
    "global_store_dwordx4 v240, v[72:75], s[52:53] sc1\n\t" \
    "s_mov_b32 exec_hi, 0\n\t" \
    "global_store_dwordx2 v241, v[76:77], s[52:53] sc1\n\t" \
    "s_mov_b32 exec_hi, -1\n\t" \
    "v_pk_add_f32 v[32:33], v[32:33], v[34:35]\n\t" \
    "v_pk_add_f32 v[36:37], v[36:37], v[38:39]\n\t" \
    "v_pk_add_f32 v[40:41], v[40:41], v[42:43]\n\t" \
    "v_cmp_nlt_f32 s[54:55], s56, v44\n\t" \
    "v_pk_add_f32 v[32:33], v[32:33], v[36:37]\n\t" \
    "v_cmp_ngt_f32 vcc, s57, v44\n\t" \
    "v_pk_add_f32 v[32:33], v[32:33], v[40:41]\n\t" \
    "s_or_b64 s[54:55], s[54:55], vcc\n\t" \
    "v_add_f32 v32, v32, v33\n\t" \
    "v_cmp_ngt_f32 vcc, s57, |v32|\n\t" \
    "s_nop 0\n\t" \
    "s_or_b64 s[54:55], s[54:55], vcc\n\t" \
    "v_pk_add_f32 v[48:49], v[48:49], v[50:51]\n\t" \
    "v_pk_add_f32 v[52:53], v[52:53], v[54:55]\n\t" \
    "v_pk_add_f32 v[56:57], v[56:57], v[58:59]\n\t" \
    "v_cmp_nlt_f32 s[66:67], s56, v60\n\t" \
    "v_pk_add_f32 v[48:49], v[48:49], v[52:53]\n\t" \
    "v_cmp_ngt_f32 vcc, s57, v60\n\t" \
    "v_pk_add_f32 v[48:49], v[48:49], v[56:57]\n\t" \
    "s_or_b64 s[66:67], s[66:67], vcc\n\t" \
    "v_add_f32 v48, v48, v49\n\t" \
    "v_cmp_ngt_f32 vcc, s57, |v48|\n\t" \
    "s_nop 0\n\t" \
    "s_or_b64 s[66:67], s[66:67], vcc\n\t" \
    "s_branch 2f\n\t" \
    "10:\n\t" \
    "s_sleep 1\n\t" \
    "s_add_u32 s49, s49, -1\n\t" \
    "s_cbranch_scc0 11b\n\t" \
    "ds_read_b32 v172, v170\n\t" \
    "s_waitcnt lgkmcnt(0)\n\t" \
    "v_cmp_le_u32 vcc, v173, v172\n\t" \
    "s_cbranch_vccz 10b\n\t" \
    "s_branch 11b\n\t" \
    "12:\n\t" \
    "s_sleep 1\n\t" \
    "s_add_u32 s49, s49, -1\n\t" \
    "s_cbranch_scc0 13b\n\t" \
    "ds_read_b32 v172, v170\n\t" \
    "s_waitcnt lgkmcnt(0)\n\t" \
    "v_cmp_le_u32 vcc, v173, v172\n\t" \
    "s_cbranch_vccz 12b\n\t" \
    "s_branch 13b\n\t" \
    "14:\n\t" \
    "s_sleep 1\n\t" \
    "s_add_u32 s49, s49, -1\n\t" \
    "s_cbranch_scc0 15b\n\t" \
    "ds_read_b32 v172, v170\n\t" \
    "s_waitcnt lgkmcnt(0)\n\t" \
    "v_cmp_le_u32 vcc, v173, v172\n\t" \
    "s_cbranch_vccz 14b\n\t" \
    "s_branch 15b\n\t" \
    "16:\n\t" \
    "s_sleep 1\n\t" \
    "s_add_u32 s49, s49, -1\n\t" \
    "s_cbranch_scc0 17b\n\t" \
    "ds_read_b32 v172, v170\n\t" \
    "s_waitcnt lgkmcnt(0)\n\t" \
    "v_cmp_le_u32 vcc, v173, v172\n\t" \
    "s_cbranch_vccz 16b\n\t" \
    "s_branch 17b\n\t" \
    "5:\n\t" \
    "s_barrier\n\t" \
    "ds_write_b128 v174, v[0:3] offset:0\n\t" \
    "ds_write_b128 v174, v[4:7] offset:1024\n\t" \
    "ds_write_b128 v174, v[8:11] offset:2048\n\t" \
    "ds_write_b128 v174, v[16:19] offset:3072\n\t" \
    "ds_write_b128 v174, v[20:23] offset:4096\n\t" \
    "ds_write_b128 v174, v[24:27] offset:5120\n\t" \
    "s_branch 6b\n\t" \
    "2:\n\t" \
    ""
#define UP_ASM \
    "s_mov_b32 s47, m0\n\t" \
    "v_add_u32 v90, 40960, v89\n\t" \
    "v_add_u32 v94, 81920, v89\n\t" \
    "s_waitcnt vmcnt(3)\n\t" \
    "s_barrier\n\t" \
    "ds_read_b128 v[32:35], v89 offset:0\n\t" \
    "ds_read_b128 v[36:39], v89 offset:256\n\t" \
    "ds_read_b128 v[40:43], v89 offset:2048\n\t" \
    "ds_read_b128 v[44:47], v89 offset:2304\n\t" \
    "ds_read_b128 v[48:51], v89 offset:4096\n\t" \
    "ds_read_b128 v[52:55], v89 offset:4352\n\t" \
    "s_waitcnt lgkmcnt(5)\n\t" \
    "v_mfma_f32_16x16x32_bf16 v[0:3], v[96:99], v[32:35], 0\n\t" \
    "ds_read_b128 v[56:59], v89 offset:6144\n\t" \
    "v_mfma_f32_16x16x32_bf16 v[4:7], v[100:103], v[32:35], 0\n\t" \
    "s_add_u32 m0, s43, 43008\n\t" \
    "s_add_u32 s48, s40, 43008\n\t" \
    "s_addc_u32 s49, s41, 0\n\t" \
    "global_load_lds_dwordx4 v91, s[48:49] offset:-2048 sc1\n\t" \
    "s_waitcnt lgkmcnt(5)\n\t" \
    "v_mfma_f32_16x16x32_bf16 v[8:11], v[96:99], v[36:39], 0\n\t" \
    "ds_read_b128 v[60:63], v89 offset:6400\n\t" \
    "v_mfma_f32_16x16x32_bf16 v[12:15], v[100:103], v[36:39], 0\n\t" \
    "s_waitcnt lgkmcnt(5)\n\t" \
    "v_mfma_f32_16x16x32_bf16 v[0:3], v[104:107], v[40:43], v[0:3]\n\t" \
    "ds_read_b128 v[32:35], v89 offset:8192\n\t" \
    "v_mfma_f32_16x16x32_bf16 v[4:7], v[108:111], v[40:43], v[4:7]\n\t" \
    "global_load_lds_dwordx4 v91, s[48:49] offset:-1024 sc1\n\t" \
    "s_waitcnt lgkmcnt(5)\n\t" \
    "v_mfma_f32_16x16x32_bf16 v[8:11], v[104:107], v[44:47], v[8:11]\n\t" \
    "ds_read_b128 v[36:39], v89 offset:8448\n\t" \
    "v_mfma_f32_16x16x32_bf16 v[12:15], v[108:111], v[44:47], v[12:15]\n\t" \
    "s_waitcnt lgkmcnt(5)\n\t" \
    "v_mfma_f32_16x16x32_bf16 v[0:3], v[112:115], v[48:51], v[0:3]\n\t" \
    "ds_read_b128 v[40:43], v89 offset:10240\n\t" \
    "v_mfma_f32_16x16x32_bf16 v[4:7], v[116:119], v[48:51], v[4:7]\n\t" \
    "global_load_lds_dwordx4 v91, s[48:49] offset:0 sc1\n\t" \
    "s_waitcnt lgkmcnt(5)\n\t" \
    "v_mfma_f32_16x16x32_bf16 v[8:11], v[112:115], v[52:55], v[8:11]\n\t" \
    "ds_read_b128 v[44:47], v89 offset:10496\n\t" \
    "v_mfma_f32_16x16x32_bf16 v[12:15], v[116:119], v[52:55], v[12:15]\n\t" \
    "s_waitcnt lgkmcnt(5)\n\t" \
    "v_mfma_f32_16x16x32_bf16 v[0:3], v[120:123], v[56:59], v[0:3]\n\t" \
    "ds_read_b128 v[48:51], v89 offset:12288\n\t" \
    "v_mfma_f32_16x16x32_bf16 v[4:7], v[124:127], v[56:59], v[4:7]\n\t" \
    "global_load_lds_dwordx4 v91, s[48:49] offset:1024 sc1\n\t" \
    "s_waitcnt lgkmcnt(5)\n\t" \
    "v_mfma_f32_16x16x32_bf16 v[8:11], v[120:123], v[60:63], v[8:11]\n\t" \
    "ds_read_b128 v[52:55], v89 offset:12544\n\t" \
    "v_mfma_f32_16x16x32_bf16 v[12:15], v[124:127], v[60:63], v[12:15]\n\t" \
    "s_waitcnt lgkmcnt(5)\n\t" \
    "v_mfma_f32_16x16x32_bf16 v[0:3], v[128:131], v[32:35], v[0:3]\n\t" \
    "ds_read_b128 v[56:59], v89 offset:14336\n\t" \
    "v_mfma_f32_16x16x32_bf16 v[4:7], v[132:135], v[32:35], v[4:7]\n\t" \
    "global_load_lds_dwordx4 v91, s[48:49] offset:2048 sc1\n\t" \
    "s_waitcnt lgkmcnt(5)\n\t" \
    "v_mfma_f32_16x16x32_bf16 v[8:11], v[128:131], v[36:39], v[8:11]\n\t" \
    "ds_read_b128 v[60:63], v89 offset:14592\n\t" \
    "v_mfma_f32_16x16x32_bf16 v[12:15], v[132:135], v[36:39], v[12:15]\n\t" \
    "s_waitcnt vmcnt(5)\n\t" \
    "s_barrier\n\t" \
    "s_waitcnt lgkmcnt(5)\n\t" \
    "v_mfma_f32_16x16x32_bf16 v[0:3], v[136:139], v[40:43], v[0:3]\n\t" \
    "ds_read_b128 v[32:35], v89 offset:16384\n\t" \
    "v_mfma_f32_16x16x32_bf16 v[4:7], v[140:143], v[40:43], v[4:7]\n\t" \
    "s_waitcnt lgkmcnt(5)\n\t" \
    "v_mfma_f32_16x16x32_bf16 v[8:11], v[136:139], v[44:47], v[8:11]\n\t" \
    "ds_read_b128 v[36:39], v89 offset:16640\n\t" \
    "v_mfma_f32_16x16x32_bf16 v[12:15], v[140:143], v[44:47], v[12:15]\n\t" \
    "s_waitcnt lgkmcnt(5)\n\t" \
    "v_mfma_f32_16x16x32_bf16 v[0:3], v[144:147], v[48:51], v[0:3]\n\t" \
    "ds_read_b128 v[40:43], v89 offset:18432\n\t" \
    "v_mfma_f32_16x16x32_bf16 v[4:7], v[148:151], v[48:51], v[4:7]\n\t" \
    "s_waitcnt lgkmcnt(5)\n\t" \
    "v_mfma_f32_16x16x32_bf16 v[8:11], v[144:147], v[52:55], v[8:11]\n\t" \
    "ds_read_b128 v[44:47], v89 offset:18688\n\t" \
    "v_mfma_f32_16x16x32_bf16 v[12:15], v[148:151], v[52:55], v[12:15]\n\t" \
    "s_waitcnt lgkmcnt(5)\n\t" \
    "v_mfma_f32_16x16x32_bf16 v[0:3], v[152:155], v[56:59], v[0:3]\n\t" \
    "ds_read_b128 v[48:51], v89 offset:20480\n\t" \
    "v_mfma_f32_16x16x32_bf16 v[4:7], v[156:159], v[56:59], v[4:7]\n\t" \
    "s_waitcnt lgkmcnt(5)\n\t" \
    "v_mfma_f32_16x16x32_bf16 v[8:11], v[152:155], v[60:63], v[8:11]\n\t" \
    "ds_read_b128 v[52:55], v89 offset:20736\n\t" \
    "v_mfma_f32_16x16x32_bf16 v[12:15], v[156:159], v[60:63], v[12:15]\n\t" \
    "s_waitcnt lgkmcnt(5)\n\t" \
    "v_mfma_f32_16x16x32_bf16 v[0:3], v[160:163], v[32:35], v[0:3]\n\t" \
    "ds_read_b128 v[56:59], v89 offset:22528\n\t" \
    "v_mfma_f32_16x16x32_bf16 v[4:7], v[164:167], v[32:35], v[4:7]\n\t" \
    "s_waitcnt lgkmcnt(5)\n\t" \
    "v_mfma_f32_16x16x32_bf16 v[8:11], v[160:163], v[36:39], v[8:11]\n\t" \
    "ds_read_b128 v[60:63], v89 offset:22784\n\t" \
    "v_mfma_f32_16x16x32_bf16 v[12:15], v[164:167], v[36:39], v[12:15]\n\t" \
    "s_waitcnt lgkmcnt(5)\n\t" \
    "v_mfma_f32_16x16x32_bf16 v[0:3], v[168:171], v[40:43], v[0:3]\n\t" \
    "ds_read_b128 v[32:35], v89 offset:24576\n\t" \
    "v_mfma_f32_16x16x32_bf16 v[4:7], v[172:175], v[40:43], v[4:7]\n\t" \
    "s_waitcnt lgkmcnt(5)\n\t" \
    "v_mfma_f32_16x16x32_bf16 v[8:11], v[168:171], v[44:47], v[8:11]\n\t" \
    "ds_read_b128 v[36:39], v89 offset:24832\n\t" \
    "v_mfma_f32_16x16x32_bf16 v[12:15], v[172:175], v[44:47], v[12:15]\n\t" \
    "s_waitcnt lgkmcnt(5)\n\t" \
    "v_mfma_f32_16x16x32_bf16 v[0:3], v[176:179], v[48:51], v[0:3]\n\t" \
    "ds_read_b128 v[40:43], v89 offset:26624\n\t" \
    "v_mfma_f32_16x16x32_bf16 v[4:7], v[180:183], v[48:51], v[4:7]\n\t" \
    "s_waitcnt vmcnt(0)\n\t" \
    "s_barrier\n\t" \
    "s_waitcnt lgkmcnt(5)\n\t" \
    "v_mfma_f32_16x16x32_bf16 v[8:11], v[176:179], v[52:55], v[8:11]\n\t" \
    "ds_read_b128 v[44:47], v89 offset:26880\n\t" \
    "v_mfma_f32_16x16x32_bf16 v[12:15], v[180:183], v[52:55], v[12:15]\n\t" \
    "s_add_u32 m0, s43, 83968\n\t" \
    "s_add_u32 s48, s40, 83968\n\t" \
    "s_addc_u32 s49, s41, 0\n\t" \
    "global_load_lds_dwordx4 v91, s[48:49] offset:-2048 sc1\n\t" \
    "s_waitcnt lgkmcnt(5)\n\t" \
    "v_mfma_f32_16x16x32_bf16 v[0:3], v[184:187], v[56:59], v[0:3]\n\t" \
    "ds_read_b128 v[48:51], v89 offset:28672\n\t" \
    "v_mfma_f32_16x16x32_bf16 v[4:7], v[188:191], v[56:59], v[4:7]\n\t" \
    "ds_read_b64 v[72:73], v93 offset:16\n\t" \
    "ds_read_b64 v[74:75], v93 offset:528\n\t" \
    "s_waitcnt lgkmcnt(7)\n\t" \
    "v_mfma_f32_16x16x32_bf16 v[8:11], v[184:187], v[60:63], v[8:11]\n\t" \
    "ds_read_b128 v[52:55], v89 offset:28928\n\t" \
    "v_mfma_f32_16x16x32_bf16 v[12:15], v[188:191], v[60:63], v[12:15]\n\t" \
    "global_load_lds_dwordx4 v91, s[48:49] offset:-1024 sc1\n\t" \
    "s_waitcnt lgkmcnt(7)\n\t" \
    "v_mfma_f32_16x16x32_bf16 v[0:3], v[192:195], v[32:35], v[0:3]\n\t" \
    "ds_read_b128 v[56:59], v89 offset:30720\n\t" \
    "v_mfma_f32_16x16x32_bf16 v[4:7], v[196:199], v[32:35], v[4:7]\n\t" \
    "s_waitcnt lgkmcnt(7)\n\t" \
    "v_mfma_f32_16x16x32_bf16 v[8:11], v[192:195], v[36:39], v[8:11]\n\t" \
    "ds_read_b128 v[60:63], v89 offset:30976\n\t" \
    "v_mfma_f32_16x16x32_bf16 v[12:15], v[196:199], v[36:39], v[12:15]\n\t" \
    "global_load_lds_dwordx4 v91, s[48:49] offset:0 sc1\n\t" \
    "s_waitcnt lgkmcnt(7)\n\t" \
    "v_mfma_f32_16x16x32_bf16 v[0:3], v[200:203], v[40:43], v[0:3]\n\t" \
    "ds_read_b128 v[32:35], v89 offset:32768\n\t" \
    "v_mfma_f32_16x16x32_bf16 v[4:7], v[204:207], v[40:43], v[4:7]\n\t" \
    "s_waitcnt lgkmcnt(7)\n\t" \
    "v_mfma_f32_16x16x32_bf16 v[8:11], v[200:203], v[44:47], v[8:11]\n\t" \
    "ds_read_b128 v[36:39], v89 offset:33024\n\t" \
    "v_mfma_f32_16x16x32_bf16 v[12:15], v[204:207], v[44:47], v[12:15]\n\t" \
    "global_load_lds_dwordx4 v91, s[48:49] offset:1024 sc1\n\t" \
    "s_waitcnt lgkmcnt(7)\n\t" \
    "v_mfma_f32_16x16x32_bf16 v[0:3], v[208:211], v[48:51], v[0:3]\n\t" \
    "ds_read_b128 v[40:43], v89 offset:34816\n\t" \
    "v_mfma_f32_16x16x32_bf16 v[4:7], v[212:215], v[48:51], v[4:7]\n\t" \
    "s_waitcnt lgkmcnt(5)\n\t" \
    "v_mfma_f32_16x16x32_bf16 v[8:11], v[208:211], v[52:55], v[8:11]\n\t" \
    "ds_read_b128 v[44:47], v89 offset:35072\n\t" \
    "v_mfma_f32_16x16x32_bf16 v[12:15], v[212:215], v[52:55], v[12:15]\n\t" \
    "global_load_lds_dwordx4 v91, s[48:49] offset:2048 sc1\n\t" \
    "s_waitcnt lgkmcnt(5)\n\t" \
    "v_mfma_f32_16x16x32_bf16 v[0:3], v[216:219], v[56:59], v[0:3]\n\t" \
    "ds_read_b128 v[48:51], v89 offset:36864\n\t" \
    "v_mfma_f32_16x16x32_bf16 v[4:7], v[220:223], v[56:59], v[4:7]\n\t" \
    "s_waitcnt lgkmcnt(5)\n\t" \
    "v_mfma_f32_16x16x32_bf16 v[8:11], v[216:219], v[60:63], v[8:11]\n\t" \
    "ds_read_b128 v[52:55], v89 offset:37120\n\t" \
    "v_mfma_f32_16x16x32_bf16 v[12:15], v[220:223], v[60:63], v[12:15]\n\t" \
    "s_waitcnt lgkmcnt(5)\n\t" \
    "v_mfma_f32_16x16x32_bf16 v[0:3], v[224:227], v[32:35], v[0:3]\n\t" \
    "ds_read_b128 v[56:59], v89 offset:38912\n\t" \
    "v_mfma_f32_16x16x32_bf16 v[4:7], v[228:231], v[32:35], v[4:7]\n\t" \
    "s_waitcnt lgkmcnt(5)\n\t" \
    "v_mfma_f32_16x16x32_bf16 v[8:11], v[224:227], v[36:39], v[8:11]\n\t" \
    "ds_read_b128 v[60:63], v89 offset:39168\n\t" \
    "v_mfma_f32_16x16x32_bf16 v[12:15], v[228:231], v[36:39], v[12:15]\n\t" \
    "s_waitcnt lgkmcnt(5)\n\t" \
    "v_mfma_f32_16x16x32_bf16 v[0:3], v[232:235], v[40:43], v[0:3]\n\t" \
    "ds_read_b128 v[32:35], v90 offset:0\n\t" \
    "v_mfma_f32_16x16x32_bf16 v[4:7], v[236:239], v[40:43], v[4:7]\n\t" \
    "s_waitcnt lgkmcnt(5)\n\t" \
    "v_mfma_f32_16x16x32_bf16 v[8:11], v[232:235], v[44:47], v[8:11]\n\t" \
    "ds_read_b128 v[36:39], v90 offset:256\n\t" \
    "v_mfma_f32_16x16x32_bf16 v[12:15], v[236:239], v[44:47], v[12:15]\n\t" \
    "s_waitcnt lgkmcnt(5)\n\t" \
    "v_mfma_f32_16x16x32_bf16 v[0:3], v[240:243], v[48:51], v[0:3]\n\t" \
    "ds_read_b128 v[40:43], v90 offset:2048\n\t" \
    "v_mfma_f32_16x16x32_bf16 v[4:7], v[244:247], v[48:51], v[4:7]\n\t" \
    "s_waitcnt lgkmcnt(5)\n\t" \
    "v_mfma_f32_16x16x32_bf16 v[8:11], v[240:243], v[52:55], v[8:11]\n\t" \
    "ds_read_b128 v[44:47], v90 offset:2304\n\t" \
    "v_mfma_f32_16x16x32_bf16 v[12:15], v[244:247], v[52:55], v[12:15]\n\t" \
    "ds_read_b96 v[64:66], v93 offset:0\n\t" \
    "ds_read_b96 v[68:70], v93 offset:512\n\t" \
    "s_waitcnt lgkmcnt(7)\n\t" \
    "v_mfma_f32_16x16x32_bf16 v[0:3], v[248:251], v[56:59], v[0:3]\n\t" \
    "ds_read_b128 v[48:51], v90 offset:4096\n\t" \
    "v_mfma_f32_16x16x32_bf16 v[4:7], v[252:255], v[56:59], v[4:7]\n\t" \
    "s_waitcnt lgkmcnt(7)\n\t" \
    "v_mfma_f32_16x16x32_bf16 v[8:11], v[248:251], v[60:63], v[8:11]\n\t" \
    "ds_read_b128 v[52:55], v90 offset:4352\n\t" \
    "v_mfma_f32_16x16x32_bf16 v[12:15], v[252:255], v[60:63], v[12:15]\n\t" \
    "v_mfma_f32_16x16x16_bf16 v[16:19], v[78:79], v[72:73], v[0:3]\n\t" \
    "v_mfma_f32_16x16x16_bf16 v[20:23], v[80:81], v[72:73], v[4:7]\n\t" \
    "v_mfma_f32_16x16x16_bf16 v[24:27], v[78:79], v[74:75], v[8:11]\n\t" \
    "v_mfma_f32_16x16x16_bf16 v[28:31], v[80:81], v[74:75], v[12:15]\n\t" \
    "s_waitcnt lgkmcnt(7)\n\t" \
    "v_mfma_f32_16x16x32_bf16 v[0:3], v[96:99], v[32:35], 0\n\t" \
    "ds_read_b128 v[56:59], v90 offset:6144\n\t" \
    "v_mfma_f32_16x16x32_bf16 v[4:7], v[100:103], v[32:35], 0\n\t" \
    "s_waitcnt lgkmcnt(7)\n\t" \
    "v_mfma_f32_16x16x32_bf16 v[8:11], v[96:99], v[36:39], 0\n\t" \
    "ds_read_b128 v[60:63], v90 offset:6400\n\t" \
    "v_mfma_f32_16x16x32_bf16 v[12:15], v[100:103], v[36:39], 0\n\t" \
    "s_waitcnt lgkmcnt(7)\n\t" \
    "v_mfma_f32_16x16x32_bf16 v[0:3], v[104:107], v[40:43], v[0:3]\n\t" \
    "v_mul_f32 v86, v16, v16\n\t" \
    "ds_read_b128 v[32:35], v90 offset:8192\n\t" \
    "v_mfma_f32_16x16x32_bf16 v[4:7], v[108:111], v[40:43], v[4:7]\n\t" \
    "v_mul_f32 v87, v17, v17\n\t" \
    "s_waitcnt lgkmcnt(7)\n\t" \
    "s_waitcnt lgkmcnt(5)\n\t" \
    "v_mfma_f32_16x16x32_bf16 v[8:11], v[104:107], v[44:47], v[8:11]\n\t" \
    "v_fma_f32 v86, v86, v65, v64\n\t" \
    "ds_read_b128 v[36:39], v90 offset:8448\n\t" \
    "v_mfma_f32_16x16x32_bf16 v[12:15], v[108:111], v[44:47], v[12:15]\n\t" \
    "v_fma_f32 v87, v87, v65, v64\n\t" \
    "s_waitcnt lgkmcnt(5)\n\t" \
    "v_mfma_f32_16x16x32_bf16 v[0:3], v[112:115], v[48:51], v[0:3]\n\t" \
    "v_mul_f32 v86, v86, v16\n\t" \
    "ds_read_b128 v[40:43], v90 offset:10240\n\t" \
    "v_mfma_f32_16x16x32_bf16 v[4:7], v[116:119], v[48:51], v[4:7]\n\t" \
    "v_mul_f32 v87, v87, v17\n\t" \
    "s_waitcnt lgkmcnt(5)\n\t" \
    "v_mfma_f32_16x16x32_bf16 v[8:11], v[112:115], v[52:55], v[8:11]\n\t" \
    "v_exp_f32 v86, v86\n\t" \
    "ds_read_b128 v[44:47], v90 offset:10496\n\t" \
    "v_mfma_f32_16x16x32_bf16 v[12:15], v[116:119], v[52:55], v[12:15]\n\t" \
    "v_exp_f32 v87, v87\n\t" \
    "s_waitcnt lgkmcnt(5)\n\t" \
    "v_mfma_f32_16x16x32_bf16 v[0:3], v[120:123], v[56:59], v[0:3]\n\t" \
    "v_mul_f32 v20, v16, v20\n\t" \
    "ds_read_b128 v[48:51], v90 offset:12288\n\t" \
    "v_mfma_f32_16x16x32_bf16 v[4:7], v[124:127], v[56:59], v[4:7]\n\t" \
    "v_fma_f32 v86, v86, v66, v66\n\t" \
    "s_waitcnt lgkmcnt(5)\n\t" \
    "v_mfma_f32_16x16x32_bf16 v[8:11], v[120:123], v[60:63], v[8:11]\n\t" \
    "v_fma_f32 v87, v87, v66, v66\n\t" \
    "ds_read_b128 v[52:55], v90 offset:12544\n\t" \
    "v_mfma_f32_16x16x32_bf16 v[12:15], v[124:127], v[60:63], v[12:15]\n\t" \
    "v_rcp_f32 v86, v86\n\t" \
    "s_waitcnt lgkmcnt(5)\n\t" \
    "v_mfma_f32_16x16x32_bf16 v[0:3], v[128:131], v[32:35], v[0:3]\n\t" \
    "v_rcp_f32 v87, v87\n\t" \
    "ds_read_b128 v[56:59], v90 offset:14336\n\t" \
    "v_mfma_f32_16x16x32_bf16 v[4:7], v[132:135], v[32:35], v[4:7]\n\t" \
    "v_mul_f32 v21, v17, v21\n\t" \
    "s_waitcnt lgkmcnt(5)\n\t" \
    "v_mfma_f32_16x16x32_bf16 v[8:11], v[128:131], v[36:39], v[8:11]\n\t" \
    "v_mul_f32 v20, v20, v86\n\t" \
    "ds_read_b128 v[60:63], v90 offset:14592\n\t" \
    "v_mfma_f32_16x16x32_bf16 v[12:15], v[132:135], v[36:39], v[12:15]\n\t" \
    "v_mul_f32 v21, v21, v87\n\t" \
    "s_waitcnt lgkmcnt(5)\n\t" \
    "v_mfma_f32_16x16x32_bf16 v[0:3], v[136:139], v[40:43], v[0:3]\n\t" \
    "v_cvt_pk_bf16_f32 v82, v20, v21\n\t" \
    "ds_read_b128 v[32:35], v90 offset:16384\n\t" \
    "v_mfma_f32_16x16x32_bf16 v[4:7], v[140:143], v[40:43], v[4:7]\n\t" \
    "v_mul_f32 v86, v18, v18\n\t" \
    "s_waitcnt lgkmcnt(5)\n\t" \
    "v_mfma_f32_16x16x32_bf16 v[8:11], v[136:139], v[44:47], v[8:11]\n\t" \
    "v_mul_f32 v87, v19, v19\n\t" \
    "ds_read_b128 v[36:39], v90 offset:16640\n\t" \
    "v_mfma_f32_16x16x32_bf16 v[12:15], v[140:143], v[44:47], v[12:15]\n\t" \
    "v_fma_f32 v86, v86, v65, v64\n\t" \
    "s_waitcnt lgkmcnt(5)\n\t" \
    "v_mfma_f32_16x16x32_bf16 v[0:3], v[144:147], v[48:51], v[0:3]\n\t" \
    "v_fma_f32 v87, v87, v65, v64\n\t" \
    "ds_read_b128 v[40:43], v90 offset:18432\n\t" \
    "v_mfma_f32_16x16x32_bf16 v[4:7], v[148:151], v[48:51], v[4:7]\n\t" \
    "v_mul_f32 v86, v86, v18\n\t" \
    "s_waitcnt lgkmcnt(5)\n\t" \
    "v_mfma_f32_16x16x32_bf16 v[8:11], v[144:147], v[52:55], v[8:11]\n\t" \
    "v_mul_f32 v87, v87, v19\n\t" \
    "ds_read_b128 v[44:47], v90 offset:18688\n\t" \
    "v_mfma_f32_16x16x32_bf16 v[12:15], v[148:151], v[52:55], v[12:15]\n\t" \
    "v_exp_f32 v86, v86\n\t" \
    "s_waitcnt lgkmcnt(5)\n\t" \
    "v_mfma_f32_16x16x32_bf16 v[0:3], v[152:155], v[56:59], v[0:3]\n\t" \
    "v_exp_f32 v87, v87\n\t" \
    "ds_read_b128 v[48:51], v90 offset:20480\n\t" \
    "v_mfma_f32_16x16x32_bf16 v[4:7], v[156:159], v[56:59], v[4:7]\n\t" \
    "v_mul_f32 v22, v18, v22\n\t" \
    "s_waitcnt lgkmcnt(5)\n\t" \
    "v_mfma_f32_16x16x32_bf16 v[8:11], v[152:155], v[60:63], v[8:11]\n\t" \
    "v_fma_f32 v86, v86, v66, v66\n\t" \
    "ds_read_b128 v[52:55], v90 offset:20736\n\t" \
    "v_mfma_f32_16x16x32_bf16 v[12:15], v[156:159], v[60:63], v[12:15]\n\t" \
    "v_fma_f32 v87, v87, v66, v66\n\t" \
    "s_waitcnt lgkmcnt(5)\n\t" \
    "v_mfma_f32_16x16x32_bf16 v[0:3], v[160:163], v[32:35], v[0:3]\n\t" \
    "v_rcp_f32 v86, v86\n\t" \
    "ds_read_b128 v[56:59], v90 offset:22528\n\t" \
    "v_mfma_f32_16x16x32_bf16 v[4:7], v[164:167], v[32:35], v[4:7]\n\t" \
    "v_rcp_f32 v87, v87\n\t" \
    "s_waitcnt lgkmcnt(5)\n\t" \
    "v_mfma_f32_16x16x32_bf16 v[8:11], v[160:163], v[36:39], v[8:11]\n\t" \
    "v_mul_f32 v23, v19, v23\n\t" \
    "ds_read_b128 v[60:63], v90 offset:22784\n\t" \
    "v_mfma_f32_16x16x32_bf16 v[12:15], v[164:167], v[36:39], v[12:15]\n\t" \
    "v_mul_f32 v22, v22, v86\n\t" \
    "s_waitcnt lgkmcnt(5)\n\t" \
    "v_mfma_f32_16x16x32_bf16 v[0:3], v[168:171], v[40:43], v[0:3]\n\t" \
    "v_mul_f32 v23, v23, v87\n\t" \
    "ds_read_b128 v[32:35], v90 offset:24576\n\t" \
    "v_mfma_f32_16x16x32_bf16 v[4:7], v[172:175], v[40:43], v[4:7]\n\t" \
    "v_cvt_pk_bf16_f32 v83, v22, v23\n\t" \
    "s_waitcnt lgkmcnt(5)\n\t" \
    "v_mfma_f32_16x16x32_bf16 v[8:11], v[168:171], v[44:47], v[8:11]\n\t" \
    "v_mul_f32 v86, v24, v24\n\t" \
    "ds_read_b128 v[36:39], v90 offset:24832\n\t" \
    "v_mfma_f32_16x16x32_bf16 v[12:15], v[172:175], v[44:47], v[12:15]\n\t" \
    "v_mul_f32 v87, v25, v25\n\t" \
    "s_waitcnt lgkmcnt(5)\n\t" \
    "v_mfma_f32_16x16x32_bf16 v[0:3], v[176:179], v[48:51], v[0:3]\n\t" \
    "v_fma_f32 v86, v86, v69, v68\n\t" \
    "ds_read_b128 v[40:43], v90 offset:26624\n\t" \
    "v_mfma_f32_16x16x32_bf16 v[4:7], v[180:183], v[48:51], v[4:7]\n\t" \
    "v_fma_f32 v87, v87, v69, v68\n\t" \
    "s_waitcnt vmcnt(0)\n\t" \
    "s_barrier\n\t" \
    "s_waitcnt lgkmcnt(5)\n\t" \
    "v_mfma_f32_16x16x32_bf16 v[8:11], v[176:179], v[52:55], v[8:11]\n\t" \
    "v_mul_f32 v86, v86, v24\n\t" \
    "ds_read_b128 v[44:47], v90 offset:26880\n\t" \
    "v_mfma_f32_16x16x32_bf16 v[12:15], v[180:183], v[52:55], v[12:15]\n\t" \
    "v_mul_f32 v87, v87, v25\n\t" \
    "s_add_u32 m0, s43, 2048\n\t" \
    "s_add_u32 s48, s40, 124928\n\t" \
    "s_addc_u32 s49, s41, 0\n\t" \
    "global_load_lds_dwordx4 v91, s[48:49] offset:-2048 sc1\n\t" \
    "s_waitcnt lgkmcnt(5)\n\t" \
    "v_mfma_f32_16x16x32_bf16 v[0:3], v[184:187], v[56:59], v[0:3]\n\t" \
    "v_exp_f32 v86, v86\n\t" \
    "ds_read_b128 v[48:51], v90 offset:28672\n\t" \
    "v_mfma_f32_16x16x32_bf16 v[4:7], v[188:191], v[56:59], v[4:7]\n\t" \
    "v_exp_f32 v87, v87\n\t" \
    "ds_read_b64 v[72:73], v93 offset:1040\n\t" \
    "ds_read_b64 v[74:75], v93 offset:1552\n\t" \
    "s_waitcnt lgkmcnt(7)\n\t" \
    "v_mfma_f32_16x16x32_bf16 v[8:11], v[184:187], v[60:63], v[8:11]\n\t" \
    "v_mul_f32 v28, v24, v28\n\t" \
    "ds_read_b128 v[52:55], v90 offset:28928\n\t" \
    "v_mfma_f32_16x16x32_bf16 v[12:15], v[188:191], v[60:63], v[12:15]\n\t" \
    "v_fma_f32 v86, v86, v70, v70\n\t" \
    "global_load_lds_dwordx4 v91, s[48:49] offset:-1024 sc1\n\t" \
    "s_waitcnt lgkmcnt(7)\n\t" \
    "v_mfma_f32_16x16x32_bf16 v[0:3], v[192:195], v[32:35], v[0:3]\n\t" \
    "v_fma_f32 v87, v87, v70, v70\n\t" \
    "ds_read_b128 v[56:59], v90 offset:30720\n\t" \
    "v_mfma_f32_16x16x32_bf16 v[4:7], v[196:199], v[32:35], v[4:7]\n\t" \
    "v_rcp_f32 v86, v86\n\t" \
    "s_waitcnt lgkmcnt(7)\n\t" \
    "v_mfma_f32_16x16x32_bf16 v[8:11], v[192:195], v[36:39], v[8:11]\n\t" \
    "v_rcp_f32 v87, v87\n\t" \
    "ds_read_b128 v[60:63], v90 offset:30976\n\t" \
    "v_mfma_f32_16x16x32_bf16 v[12:15], v[196:199], v[36:39], v[12:15]\n\t" \
    "v_mul_f32 v29, v25, v29\n\t" \
    "global_load_lds_dwordx4 v91, s[48:49] offset:0 sc1\n\t" \
    "s_waitcnt lgkmcnt(7)\n\t" \
    "v_mfma_f32_16x16x32_bf16 v[0:3], v[200:203], v[40:43], v[0:3]\n\t" \
    "v_mul_f32 v28, v28, v86\n\t" \
    "ds_read_b128 v[32:35], v90 offset:32768\n\t" \
    "v_mfma_f32_16x16x32_bf16 v[4:7], v[204:207], v[40:43], v[4:7]\n\t" \
    "v_mul_f32 v29, v29, v87\n\t" \
    "s_waitcnt lgkmcnt(7)\n\t" \
    "v_mfma_f32_16x16x32_bf16 v[8:11], v[200:203], v[44:47], v[8:11]\n\t" \
    "v_cvt_pk_bf16_f32 v84, v28, v29\n\t" \
    "ds_read_b128 v[36:39], v90 offset:33024\n\t" \
    "v_mfma_f32_16x16x32_bf16 v[12:15], v[204:207], v[44:47], v[12:15]\n\t" \
    "v_mul_f32 v86, v26, v26\n\t" \
    "global_load_lds_dwordx4 v91, s[48:49] offset:1024 sc1\n\t" \
    "s_waitcnt lgkmcnt(7)\n\t" \
    "v_mfma_f32_16x16x32_bf16 v[0:3], v[208:211], v[48:51], v[0:3]\n\t" \
    "v_mul_f32 v87, v27, v27\n\t" \
    "ds_read_b128 v[40:43], v90 offset:34816\n\t" \
    "v_mfma_f32_16x16x32_bf16 v[4:7], v[212:215], v[48:51], v[4:7]\n\t" \
    "v_fma_f32 v86, v86, v69, v68\n\t" \
    "s_waitcnt lgkmcnt(5)\n\t" \
    "v_mfma_f32_16x16x32_bf16 v[8:11], v[208:211], v[52:55], v[8:11]\n\t" \
    "v_fma_f32 v87, v87, v69, v68\n\t" \
    "ds_read_b128 v[44:47], v90 offset:35072\n\t" \
    "v_mfma_f32_16x16x32_bf16 v[12:15], v[212:215], v[52:55], v[12:15]\n\t" \
    "v_mul_f32 v86, v86, v26\n\t" \
    "global_load_lds_dwordx4 v91, s[48:49] offset:2048 sc1\n\t" \
    "s_waitcnt lgkmcnt(5)\n\t" \
    "v_mfma_f32_16x16x32_bf16 v[0:3], v[216:219], v[56:59], v[0:3]\n\t" \
    "v_mul_f32 v87, v87, v27\n\t" \
    "ds_read_b128 v[48:51], v90 offset:36864\n\t" \
    "v_mfma_f32_16x16x32_bf16 v[4:7], v[220:223], v[56:59], v[4:7]\n\t" \
    "v_exp_f32 v86, v86\n\t" \
    "s_waitcnt lgkmcnt(5)\n\t" \
    "v_mfma_f32_16x16x32_bf16 v[8:11], v[216:219], v[60:63], v[8:11]\n\t" \
    "v_exp_f32 v87, v87\n\t" \
    "ds_read_b128 v[52:55], v90 offset:37120\n\t" \
    "v_mfma_f32_16x16x32_bf16 v[12:15], v[220:223], v[60:63], v[12:15]\n\t" \
    "v_mul_f32 v30, v26, v30\n\t" \
    "s_waitcnt lgkmcnt(5)\n\t" \
    "v_mfma_f32_16x16x32_bf16 v[0:3], v[224:227], v[32:35], v[0:3]\n\t" \
    "v_fma_f32 v86, v86, v70, v70\n\t" \
    "ds_read_b128 v[56:59], v90 offset:38912\n\t" \
    "v_mfma_f32_16x16x32_bf16 v[4:7], v[228:231], v[32:35], v[4:7]\n\t" \
    "v_fma_f32 v87, v87, v70, v70\n\t" \
    "s_waitcnt lgkmcnt(5)\n\t" \
    "v_mfma_f32_16x16x32_bf16 v[8:11], v[224:227], v[36:39], v[8:11]\n\t" \
    "v_rcp_f32 v86, v86\n\t" \
    "ds_read_b128 v[60:63], v90 offset:39168\n\t" \
    "v_mfma_f32_16x16x32_bf16 v[12:15], v[228:231], v[36:39], v[12:15]\n\t" \
    "v_rcp_f32 v87, v87\n\t" \
    "s_waitcnt lgkmcnt(5)\n\t" \
    "v_mfma_f32_16x16x32_bf16 v[0:3], v[232:235], v[40:43], v[0:3]\n\t" \
    "v_mul_f32 v31, v27, v31\n\t" \
    "ds_read_b128 v[32:35], v94 offset:0\n\t" \
    "v_mfma_f32_16x16x32_bf16 v[4:7], v[236:239], v[40:43], v[4:7]\n\t" \
    "v_mul_f32 v30, v30, v86\n\t" \
    "s_waitcnt lgkmcnt(5)\n\t" \
    "v_mfma_f32_16x16x32_bf16 v[8:11], v[232:235], v[44:47], v[8:11]\n\t" \
    "v_mul_f32 v31, v31, v87\n\t" \
    "ds_read_b128 v[36:39], v94 offset:256\n\t" \
    "v_mfma_f32_16x16x32_bf16 v[12:15], v[236:239], v[44:47], v[12:15]\n\t" \
    "v_cvt_pk_bf16_f32 v85, v30, v31\n\t" \
    "s_waitcnt lgkmcnt(5)\n\t" \
    "v_mfma_f32_16x16x32_bf16 v[0:3], v[240:243], v[48:51], v[0:3]\n\t" \
    "ds_read_b128 v[40:43], v94 offset:2048\n\t" \
    "v_mfma_f32_16x16x32_bf16 v[4:7], v[244:247], v[48:51], v[4:7]\n\t" \
    "s_waitcnt lgkmcnt(5)\n\t" \
    "v_mfma_f32_16x16x32_bf16 v[8:11], v[240:243], v[52:55], v[8:11]\n\t" \
    "ds_read_b128 v[44:47], v94 offset:2304\n\t" \
    "v_mfma_f32_16x16x32_bf16 v[12:15], v[244:247], v[52:55], v[12:15]\n\t" \
    "v_permlane16_swap_b32 v82, v84\n\t" \
    "v_permlane16_swap_b32 v83, v85\n\t" \
    "ds_read_b96 v[64:66], v93 offset:1024\n\t" \
    "ds_read_b96 v[68:70], v93 offset:1536\n\t" \
    "s_waitcnt lgkmcnt(7)\n\t" \
    "v_mfma_f32_16x16x32_bf16 v[0:3], v[248:251], v[56:59], v[0:3]\n\t" \
    "ds_read_b128 v[48:51], v94 offset:4096\n\t" \
    "v_mfma_f32_16x16x32_bf16 v[4:7], v[252:255], v[56:59], v[4:7]\n\t" \
    "global_store_dwordx4 v92, v[82:85], s[44:45] sc1\n\t" \
    "s_add_u32 s44, s44, 110592\n\t" \
    "s_addc_u32 s45, s45, 0\n\t" \
    "s_waitcnt lgkmcnt(7)\n\t" \
    "v_mfma_f32_16x16x32_bf16 v[8:11], v[248:251], v[60:63], v[8:11]\n\t" \
    "ds_read_b128 v[52:55], v94 offset:4352\n\t" \
    "v_mfma_f32_16x16x32_bf16 v[12:15], v[252:255], v[60:63], v[12:15]\n\t" \
    "v_mfma_f32_16x16x16_bf16 v[16:19], v[78:79], v[72:73], v[0:3]\n\t" \
    "v_mfma_f32_16x16x16_bf16 v[20:23], v[80:81], v[72:73], v[4:7]\n\t" \
    "v_mfma_f32_16x16x16_bf16 v[24:27], v[78:79], v[74:75], v[8:11]\n\t" \
    "v_mfma_f32_16x16x16_bf16 v[28:31], v[80:81], v[74:75], v[12:15]\n\t" \
    "s_waitcnt lgkmcnt(7)\n\t" \
    "v_mfma_f32_16x16x32_bf16 v[0:3], v[96:99], v[32:35], 0\n\t" \
    "ds_read_b128 v[56:59], v94 offset:6144\n\t" \
    "v_mfma_f32_16x16x32_bf16 v[4:7], v[100:103], v[32:35], 0\n\t" \
    "s_waitcnt lgkmcnt(7)\n\t" \
    "v_mfma_f32_16x16x32_bf16 v[8:11], v[96:99], v[36:39], 0\n\t" \
    "ds_read_b128 v[60:63], v94 offset:6400\n\t" \
    "v_mfma_f32_16x16x32_bf16 v[12:15], v[100:103], v[36:39], 0\n\t" \
    "s_waitcnt lgkmcnt(7)\n\t" \
    "v_mfma_f32_16x16x32_bf16 v[0:3], v[104:107], v[40:43], v[0:3]\n\t" \
    "v_mul_f32 v86, v16, v16\n\t" \
    "ds_read_b128 v[32:35], v94 offset:8192\n\t" \
    "v_mfma_f32_16x16x32_bf16 v[4:7], v[108:111], v[40:43], v[4:7]\n\t" \
    "v_mul_f32 v87, v17, v17\n\t" \
    "s_waitcnt lgkmcnt(7)\n\t" \
    "s_waitcnt lgkmcnt(5)\n\t" \
    "v_mfma_f32_16x16x32_bf16 v[8:11], v[104:107], v[44:47], v[8:11]\n\t" \
    "v_fma_f32 v86, v86, v65, v64\n\t" \
    "ds_read_b128 v[36:39], v94 offset:8448\n\t" \
    "v_mfma_f32_16x16x32_bf16 v[12:15], v[108:111], v[44:47], v[12:15]\n\t" \
    "v_fma_f32 v87, v87, v65, v64\n\t" \
    "s_waitcnt lgkmcnt(5)\n\t" \
    "v_mfma_f32_16x16x32_bf16 v[0:3], v[112:115], v[48:51], v[0:3]\n\t" \
    "v_mul_f32 v86, v86, v16\n\t" \
    "ds_read_b128 v[40:43], v94 offset:10240\n\t" \
    "v_mfma_f32_16x16x32_bf16 v[4:7], v[116:119], v[48:51], v[4:7]\n\t" \
    "v_mul_f32 v87, v87, v17\n\t" \
    "s_waitcnt lgkmcnt(5)\n\t" \
    "v_mfma_f32_16x16x32_bf16 v[8:11], v[112:115], v[52:55], v[8:11]\n\t" \
    "v_exp_f32 v86, v86\n\t" \
    "ds_read_b128 v[44:47], v94 offset:10496\n\t" \
    "v_mfma_f32_16x16x32_bf16 v[12:15], v[116:119], v[52:55], v[12:15]\n\t" \
    "v_exp_f32 v87, v87\n\t" \
    "s_waitcnt lgkmcnt(5)\n\t" \
    "v_mfma_f32_16x16x32_bf16 v[0:3], v[120:123], v[56:59], v[0:3]\n\t" \
    "v_mul_f32 v20, v16, v20\n\t" \
    "ds_read_b128 v[48:51], v94 offset:12288\n\t" \
    "v_mfma_f32_16x16x32_bf16 v[4:7], v[124:127], v[56:59], v[4:7]\n\t" \
    "v_fma_f32 v86, v86, v66, v66\n\t" \
    "s_waitcnt lgkmcnt(5)\n\t" \
    "v_mfma_f32_16x16x32_bf16 v[8:11], v[120:123], v[60:63], v[8:11]\n\t" \
    "v_fma_f32 v87, v87, v66, v66\n\t" \
    "ds_read_b128 v[52:55], v94 offset:12544\n\t" \
    "v_mfma_f32_16x16x32_bf16 v[12:15], v[124:127], v[60:63], v[12:15]\n\t" \
    "v_rcp_f32 v86, v86\n\t" \
    "s_waitcnt lgkmcnt(5)\n\t" \
    "v_mfma_f32_16x16x32_bf16 v[0:3], v[128:131], v[32:35], v[0:3]\n\t" \
    "v_rcp_f32 v87, v87\n\t" \
    "ds_read_b128 v[56:59], v94 offset:14336\n\t" \
    "v_mfma_f32_16x16x32_bf16 v[4:7], v[132:135], v[32:35], v[4:7]\n\t" \
    "v_mul_f32 v21, v17, v21\n\t" \
    "s_waitcnt lgkmcnt(5)\n\t" \
    "v_mfma_f32_16x16x32_bf16 v[8:11], v[128:131], v[36:39], v[8:11]\n\t" \
    "v_mul_f32 v20, v20, v86\n\t" \
    "ds_read_b128 v[60:63], v94 offset:14592\n\t" \
    "v_mfma_f32_16x16x32_bf16 v[12:15], v[132:135], v[36:39], v[12:15]\n\t" \
    "v_mul_f32 v21, v21, v87\n\t" \
    "s_waitcnt lgkmcnt(5)\n\t" \
    "v_mfma_f32_16x16x32_bf16 v[0:3], v[136:139], v[40:43], v[0:3]\n\t" \
    "v_cvt_pk_bf16_f32 v82, v20, v21\n\t" \
    "ds_read_b128 v[32:35], v94 offset:16384\n\t" \
    "v_mfma_f32_16x16x32_bf16 v[4:7], v[140:143], v[40:43], v[4:7]\n\t" \
    "v_mul_f32 v86, v18, v18\n\t" \
    "s_waitcnt lgkmcnt(5)\n\t" \
    "v_mfma_f32_16x16x32_bf16 v[8:11], v[136:139], v[44:47], v[8:11]\n\t" \
    "v_mul_f32 v87, v19, v19\n\t" \
    "ds_read_b128 v[36:39], v94 offset:16640\n\t" \
    "v_mfma_f32_16x16x32_bf16 v[12:15], v[140:143], v[44:47], v[12:15]\n\t" \
    "v_fma_f32 v86, v86, v65, v64\n\t" \
    "s_waitcnt lgkmcnt(5)\n\t" \
    "v_mfma_f32_16x16x32_bf16 v[0:3], v[144:147], v[48:51], v[0:3]\n\t" \
    "v_fma_f32 v87, v87, v65, v64\n\t" \
    "ds_read_b128 v[40:43], v94 offset:18432\n\t" \
    "v_mfma_f32_16x16x32_bf16 v[4:7], v[148:151], v[48:51], v[4:7]\n\t" \
    "v_mul_f32 v86, v86, v18\n\t" \
    "s_waitcnt lgkmcnt(5)\n\t" \
    "v_mfma_f32_16x16x32_bf16 v[8:11], v[144:147], v[52:55], v[8:11]\n\t" \
    "v_mul_f32 v87, v87, v19\n\t" \
    "ds_read_b128 v[44:47], v94 offset:18688\n\t" \
    "v_mfma_f32_16x16x32_bf16 v[12:15], v[148:151], v[52:55], v[12:15]\n\t" \
    "v_exp_f32 v86, v86\n\t" \
    "s_waitcnt lgkmcnt(5)\n\t" \
    "v_mfma_f32_16x16x32_bf16 v[0:3], v[152:155], v[56:59], v[0:3]\n\t" \
    "v_exp_f32 v87, v87\n\t" \
    "ds_read_b128 v[48:51], v94 offset:20480\n\t" \
    "v_mfma_f32_16x16x32_bf16 v[4:7], v[156:159], v[56:59], v[4:7]\n\t" \
    "v_mul_f32 v22, v18, v22\n\t" \
    "s_waitcnt lgkmcnt(5)\n\t" \
    "v_mfma_f32_16x16x32_bf16 v[8:11], v[152:155], v[60:63], v[8:11]\n\t" \
    "v_fma_f32 v86, v86, v66, v66\n\t" \
    "ds_read_b128 v[52:55], v94 offset:20736\n\t" \
    "v_mfma_f32_16x16x32_bf16 v[12:15], v[156:159], v[60:63], v[12:15]\n\t" \
    "v_fma_f32 v87, v87, v66, v66\n\t" \
    "s_waitcnt lgkmcnt(5)\n\t" \
    "v_mfma_f32_16x16x32_bf16 v[0:3], v[160:163], v[32:35], v[0:3]\n\t" \
    "v_rcp_f32 v86, v86\n\t" \
    "ds_read_b128 v[56:59], v94 offset:22528\n\t" \
    "v_mfma_f32_16x16x32_bf16 v[4:7], v[164:167], v[32:35], v[4:7]\n\t" \
    "v_rcp_f32 v87, v87\n\t" \
    "s_waitcnt lgkmcnt(5)\n\t" \
    "v_mfma_f32_16x16x32_bf16 v[8:11], v[160:163], v[36:39], v[8:11]\n\t" \
    "v_mul_f32 v23, v19, v23\n\t" \
    "ds_read_b128 v[60:63], v94 offset:22784\n\t" \
    "v_mfma_f32_16x16x32_bf16 v[12:15], v[164:167], v[36:39], v[12:15]\n\t" \
    "v_mul_f32 v22, v22, v86\n\t" \
    "s_waitcnt lgkmcnt(5)\n\t" \
    "v_mfma_f32_16x16x32_bf16 v[0:3], v[168:171], v[40:43], v[0:3]\n\t" \
    "v_mul_f32 v23, v23, v87\n\t" \
    "ds_read_b128 v[32:35], v94 offset:24576\n\t" \
    "v_mfma_f32_16x16x32_bf16 v[4:7], v[172:175], v[40:43], v[4:7]\n\t" \
    "v_cvt_pk_bf16_f32 v83, v22, v23\n\t" \
    "s_waitcnt lgkmcnt(5)\n\t" \
    "v_mfma_f32_16x16x32_bf16 v[8:11], v[168:171], v[44:47], v[8:11]\n\t" \
    "v_mul_f32 v86, v24, v24\n\t" \
    "ds_read_b128 v[36:39], v94 offset:24832\n\t" \
    "v_mfma_f32_16x16x32_bf16 v[12:15], v[172:175], v[44:47], v[12:15]\n\t" \
    "v_mul_f32 v87, v25, v25\n\t" \
    "s_waitcnt lgkmcnt(5)\n\t" \
    "v_mfma_f32_16x16x32_bf16 v[0:3], v[176:179], v[48:51], v[0:3]\n\t" \
    "v_fma_f32 v86, v86, v69, v68\n\t" \
    "ds_read_b128 v[40:43], v94 offset:26624\n\t" \
    "v_mfma_f32_16x16x32_bf16 v[4:7], v[180:183], v[48:51], v[4:7]\n\t" \
    "v_fma_f32 v87, v87, v69, v68\n\t" \
    "s_waitcnt vmcnt(0)\n\t" \
    "s_barrier\n\t" \
    "s_waitcnt lgkmcnt(5)\n\t" \
    "v_mfma_f32_16x16x32_bf16 v[8:11], v[176:179], v[52:55], v[8:11]\n\t" \
    "v_mul_f32 v86, v86, v24\n\t" \
    "ds_read_b128 v[44:47], v94 offset:26880\n\t" \
    "v_mfma_f32_16x16x32_bf16 v[12:15], v[180:183], v[52:55], v[12:15]\n\t" \
    "v_mul_f32 v87, v87, v25\n\t" \
    "s_add_u32 m0, s43, 43008\n\t" \
    "s_add_u32 s48, s54, 2048\n\t" \
    "s_addc_u32 s49, s55, 0\n\t" \
    "global_load_lds_dwordx4 v91, s[48:49] offset:-2048 sc1\n\t" \
    "s_waitcnt lgkmcnt(5)\n\t" \
    "v_mfma_f32_16x16x32_bf16 v[0:3], v[184:187], v[56:59], v[0:3]\n\t" \
    "v_exp_f32 v86, v86\n\t" \
    "ds_read_b128 v[48:51], v94 offset:28672\n\t" \
    "v_mfma_f32_16x16x32_bf16 v[4:7], v[188:191], v[56:59], v[4:7]\n\t" \
    "v_exp_f32 v87, v87\n\t" \
    "ds_read_b64 v[72:73], v93 offset:2064\n\t" \
    "ds_read_b64 v[74:75], v93 offset:2576\n\t" \
    "s_waitcnt lgkmcnt(7)\n\t" \
    "v_mfma_f32_16x16x32_bf16 v[8:11], v[184:187], v[60:63], v[8:11]\n\t" \
    "v_mul_f32 v28, v24, v28\n\t" \
    "ds_read_b128 v[52:55], v94 offset:28928\n\t" \
    "v_mfma_f32_16x16x32_bf16 v[12:15], v[188:191], v[60:63], v[12:15]\n\t" \
    "v_fma_f32 v86, v86, v70, v70\n\t" \
    "global_load_lds_dwordx4 v91, s[48:49] offset:-1024 sc1\n\t" \
    "s_waitcnt lgkmcnt(7)\n\t" \
    "v_mfma_f32_16x16x32_bf16 v[0:3], v[192:195], v[32:35], v[0:3]\n\t" \
    "v_fma_f32 v87, v87, v70, v70\n\t" \
    "ds_read_b128 v[56:59], v94 offset:30720\n\t" \
    "v_mfma_f32_16x16x32_bf16 v[4:7], v[196:199], v[32:35], v[4:7]\n\t" \
    "v_rcp_f32 v86, v86\n\t" \
    "s_waitcnt lgkmcnt(7)\n\t" \
    "v_mfma_f32_16x16x32_bf16 v[8:11], v[192:195], v[36:39], v[8:11]\n\t" \
    "v_rcp_f32 v87, v87\n\t" \
    "ds_read_b128 v[60:63], v94 offset:30976\n\t" \
    "v_mfma_f32_16x16x32_bf16 v[12:15], v[196:199], v[36:39], v[12:15]\n\t" \
    "v_mul_f32 v29, v25, v29\n\t" \
    "global_load_lds_dwordx4 v91, s[48:49] offset:0 sc1\n\t" \
    "s_waitcnt lgkmcnt(7)\n\t" \
    "v_mfma_f32_16x16x32_bf16 v[0:3], v[200:203], v[40:43], v[0:3]\n\t" \
    "v_mul_f32 v28, v28, v86\n\t" \
    "ds_read_b128 v[32:35], v94 offset:32768\n\t" \
    "v_mfma_f32_16x16x32_bf16 v[4:7], v[204:207], v[40:43], v[4:7]\n\t" \
    "v_mul_f32 v29, v29, v87\n\t" \
    "s_waitcnt lgkmcnt(7)\n\t" \
    "v_mfma_f32_16x16x32_bf16 v[8:11], v[200:203], v[44:47], v[8:11]\n\t" \
    "v_cvt_pk_bf16_f32 v84, v28, v29\n\t" \
    "ds_read_b128 v[36:39], v94 offset:33024\n\t" \
    "v_mfma_f32_16x16x32_bf16 v[12:15], v[204:207], v[44:47], v[12:15]\n\t" \
    "v_mul_f32 v86, v26, v26\n\t" \
    "global_load_lds_dwordx4 v91, s[48:49] offset:1024 sc1\n\t" \
    "s_waitcnt lgkmcnt(7)\n\t" \
    "v_mfma_f32_16x16x32_bf16 v[0:3], v[208:211], v[48:51], v[0:3]\n\t" \
    "v_mul_f32 v87, v27, v27\n\t" \
    "ds_read_b128 v[40:43], v94 offset:34816\n\t" \
    "v_mfma_f32_16x16x32_bf16 v[4:7], v[212:215], v[48:51], v[4:7]\n\t" \
    "v_fma_f32 v86, v86, v69, v68\n\t" \
    "s_waitcnt lgkmcnt(5)\n\t" \
    "v_mfma_f32_16x16x32_bf16 v[8:11], v[208:211], v[52:55], v[8:11]\n\t" \
    "v_fma_f32 v87, v87, v69, v68\n\t" \
    "ds_read_b128 v[44:47], v94 offset:35072\n\t" \
    "v_mfma_f32_16x16x32_bf16 v[12:15], v[212:215], v[52:55], v[12:15]\n\t" \
    "v_mul_f32 v86, v86, v26\n\t" \
    "global_load_lds_dwordx4 v91, s[48:49] offset:2048 sc1\n\t" \
    "s_waitcnt lgkmcnt(5)\n\t" \
    "v_mfma_f32_16x16x32_bf16 v[0:3], v[216:219], v[56:59], v[0:3]\n\t" \
    "v_mul_f32 v87, v87, v27\n\t" \
    "ds_read_b128 v[48:51], v94 offset:36864\n\t" \
    "v_mfma_f32_16x16x32_bf16 v[4:7], v[220:223], v[56:59], v[4:7]\n\t" \
    "v_exp_f32 v86, v86\n\t" \
    "s_waitcnt lgkmcnt(5)\n\t" \
    "v_mfma_f32_16x16x32_bf16 v[8:11], v[216:219], v[60:63], v[8:11]\n\t" \
    "v_exp_f32 v87, v87\n\t" \
    "ds_read_b128 v[52:55], v94 offset:37120\n\t" \
    "v_mfma_f32_16x16x32_bf16 v[12:15], v[220:223], v[60:63], v[12:15]\n\t" \
    "v_mul_f32 v30, v26, v30\n\t" \
    "s_waitcnt lgkmcnt(5)\n\t" \
    "v_mfma_f32_16x16x32_bf16 v[0:3], v[224:227], v[32:35], v[0:3]\n\t" \
    "v_fma_f32 v86, v86, v70, v70\n\t" \
    "ds_read_b128 v[56:59], v94 offset:38912\n\t" \
    "v_mfma_f32_16x16x32_bf16 v[4:7], v[228:231], v[32:35], v[4:7]\n\t" \
    "v_fma_f32 v87, v87, v70, v70\n\t" \
    "s_waitcnt lgkmcnt(5)\n\t" \
    "v_mfma_f32_16x16x32_bf16 v[8:11], v[224:227], v[36:39], v[8:11]\n\t" \
    "v_rcp_f32 v86, v86\n\t" \
    "ds_read_b128 v[60:63], v94 offset:39168\n\t" \
    "v_mfma_f32_16x16x32_bf16 v[12:15], v[228:231], v[36:39], v[12:15]\n\t" \
    "v_rcp_f32 v87, v87\n\t" \
    "s_waitcnt lgkmcnt(5)\n\t" \
    "v_mfma_f32_16x16x32_bf16 v[0:3], v[232:235], v[40:43], v[0:3]\n\t" \
    "v_mul_f32 v31, v27, v31\n\t" \
    "ds_read_b128 v[32:35], v89 offset:0\n\t" \
    "v_mfma_f32_16x16x32_bf16 v[4:7], v[236:239], v[40:43], v[4:7]\n\t" \
    "v_mul_f32 v30, v30, v86\n\t" \
    "s_waitcnt lgkmcnt(5)\n\t" \
    "v_mfma_f32_16x16x32_bf16 v[8:11], v[232:235], v[44:47], v[8:11]\n\t" \
    "v_mul_f32 v31, v31, v87\n\t" \
    "ds_read_b128 v[36:39], v89 offset:256\n\t" \
    "v_mfma_f32_16x16x32_bf16 v[12:15], v[236:239], v[44:47], v[12:15]\n\t" \
    "v_cvt_pk_bf16_f32 v85, v30, v31\n\t" \
    "s_waitcnt lgkmcnt(5)\n\t" \
    "v_mfma_f32_16x16x32_bf16 v[0:3], v[240:243], v[48:51], v[0:3]\n\t" \
    "ds_read_b128 v[40:43], v89 offset:2048\n\t" \
    "v_mfma_f32_16x16x32_bf16 v[4:7], v[244:247], v[48:51], v[4:7]\n\t" \
    "s_waitcnt lgkmcnt(5)\n\t" \
    "v_mfma_f32_16x16x32_bf16 v[8:11], v[240:243], v[52:55], v[8:11]\n\t" \
    "ds_read_b128 v[44:47], v89 offset:2304\n\t" \
    "v_mfma_f32_16x16x32_bf16 v[12:15], v[244:247], v[52:55], v[12:15]\n\t" \
    "v_permlane16_swap_b32 v82, v84\n\t" \
    "v_permlane16_swap_b32 v83, v85\n\t" \
    "ds_read_b96 v[64:66], v93 offset:2048\n\t" \
    "ds_read_b96 v[68:70], v93 offset:2560\n\t" \
    "s_waitcnt lgkmcnt(7)\n\t" \
    "v_mfma_f32_16x16x32_bf16 v[0:3], v[248:251], v[56:59], v[0:3]\n\t" \
    "ds_read_b128 v[48:51], v89 offset:4096\n\t" \
    "v_mfma_f32_16x16x32_bf16 v[4:7], v[252:255], v[56:59], v[4:7]\n\t" \
    "global_store_dwordx4 v92, v[82:85], s[44:45] sc1\n\t" \
    "s_add_u32 s44, s44, 110592\n\t" \
    "s_addc_u32 s45, s45, 0\n\t" \
    "s_waitcnt lgkmcnt(7)\n\t" \
    "v_mfma_f32_16x16x32_bf16 v[8:11], v[248:251], v[60:63], v[8:11]\n\t" \
    "ds_read_b128 v[52:55], v89 offset:4352\n\t" \
    "v_mfma_f32_16x16x32_bf16 v[12:15], v[252:255], v[60:63], v[12:15]\n\t" \
    "v_mfma_f32_16x16x16_bf16 v[16:19], v[78:79], v[72:73], v[0:3]\n\t" \
    "v_mfma_f32_16x16x16_bf16 v[20:23], v[80:81], v[72:73], v[4:7]\n\t" \
    "v_mfma_f32_16x16x16_bf16 v[24:27], v[78:79], v[74:75], v[8:11]\n\t" \
    "v_mfma_f32_16x16x16_bf16 v[28:31], v[80:81], v[74:75], v[12:15]\n\t" \
    "s_waitcnt lgkmcnt(7)\n\t" \
    "v_mfma_f32_16x16x32_bf16 v[0:3], v[96:99], v[32:35], 0\n\t" \
    "ds_read_b128 v[56:59], v89 offset:6144\n\t" \
    "v_mfma_f32_16x16x32_bf16 v[4:7], v[100:103], v[32:35], 0\n\t" \
    "s_waitcnt lgkmcnt(7)\n\t" \
    "v_mfma_f32_16x16x32_bf16 v[8:11], v[96:99], v[36:39], 0\n\t" \
    "ds_read_b128 v[60:63], v89 offset:6400\n\t" \
    "v_mfma_f32_16x16x32_bf16 v[12:15], v[100:103], v[36:39], 0\n\t" \
    "s_waitcnt lgkmcnt(7)\n\t" \
    "v_mfma_f32_16x16x32_bf16 v[0:3], v[104:107], v[40:43], v[0:3]\n\t" \
    "v_mul_f32 v86, v16, v16\n\t" \
    "ds_read_b128 v[32:35], v89 offset:8192\n\t" \
    "v_mfma_f32_16x16x32_bf16 v[4:7], v[108:111], v[40:43], v[4:7]\n\t" \
    "v_mul_f32 v87, v17, v17\n\t" \
    "s_waitcnt lgkmcnt(7)\n\t" \
    "s_waitcnt lgkmcnt(5)\n\t" \
    "v_mfma_f32_16x16x32_bf16 v[8:11], v[104:107], v[44:47], v[8:11]\n\t" \
    "v_fma_f32 v86, v86, v65, v64\n\t" \
    "ds_read_b128 v[36:39], v89 offset:8448\n\t" \
    "v_mfma_f32_16x16x32_bf16 v[12:15], v[108:111], v[44:47], v[12:15]\n\t" \
    "v_fma_f32 v87, v87, v65, v64\n\t" \
    "global_load_dwordx4 v[96:99], v91, s[56:57] offset:-4096 sc1\n\t" \
    "s_waitcnt lgkmcnt(5)\n\t" \
    "v_mfma_f32_16x16x32_bf16 v[0:3], v[112:115], v[48:51], v[0:3]\n\t" \
    "v_mul_f32 v86, v86, v16\n\t" \
    "ds_read_b128 v[40:43], v89 offset:10240\n\t" \
    "v_mfma_f32_16x16x32_bf16 v[4:7], v[116:119], v[48:51], v[4:7]\n\t" \
    "v_mul_f32 v87, v87, v17\n\t" \
    "s_waitcnt lgkmcnt(5)\n\t" \
    "v_mfma_f32_16x16x32_bf16 v[8:11], v[112:115], v[52:55], v[8:11]\n\t" \
    "v_exp_f32 v86, v86\n\t" \
    "ds_read_b128 v[44:47], v89 offset:10496\n\t" \
    "v_mfma_f32_16x16x32_bf16 v[12:15], v[116:119], v[52:55], v[12:15]\n\t" \
    "v_exp_f32 v87, v87\n\t" \
    "global_load_dwordx4 v[100:103], v91, s[56:57] offset:-3072 sc1\n\t" \
    "s_waitcnt lgkmcnt(5)\n\t" \
    "v_mfma_f32_16x16x32_bf16 v[0:3], v[120:123], v[56:59], v[0:3]\n\t" \
    "v_mul_f32 v20, v16, v20\n\t" \
    "ds_read_b128 v[48:51], v89 offset:12288\n\t" \
    "v_mfma_f32_16x16x32_bf16 v[4:7], v[124:127], v[56:59], v[4:7]\n\t" \
    "v_fma_f32 v86, v86, v66, v66\n\t" \
    "global_load_dwordx4 v[104:107], v91, s[56:57] offset:-2048 sc1\n\t" \
    "s_waitcnt lgkmcnt(5)\n\t" \
    "v_mfma_f32_16x16x32_bf16 v[8:11], v[120:123], v[60:63], v[8:11]\n\t" \
    "v_fma_f32 v87, v87, v66, v66\n\t" \
    "ds_read_b128 v[52:55], v89 offset:12544\n\t" \
    "v_mfma_f32_16x16x32_bf16 v[12:15], v[124:127], v[60:63], v[12:15]\n\t" \
    "v_rcp_f32 v86, v86\n\t" \
    "s_waitcnt lgkmcnt(5)\n\t" \
    "v_mfma_f32_16x16x32_bf16 v[0:3], v[128:131], v[32:35], v[0:3]\n\t" \
    "v_rcp_f32 v87, v87\n\t" \
    "ds_read_b128 v[56:59], v89 offset:14336\n\t" \
    "v_mfma_f32_16x16x32_bf16 v[4:7], v[132:135], v[32:35], v[4:7]\n\t" \
    "v_mul_f32 v21, v17, v21\n\t" \
    "global_load_dwordx4 v[108:111], v91, s[56:57] offset:-1024 sc1\n\t" \
    "s_waitcnt lgkmcnt(5)\n\t" \
    "v_mfma_f32_16x16x32_bf16 v[8:11], v[128:131], v[36:39], v[8:11]\n\t" \
    "v_mul_f32 v20, v20, v86\n\t" \
    "ds_read_b128 v[60:63], v89 offset:14592\n\t" \
    "v_mfma_f32_16x16x32_bf16 v[12:15], v[132:135], v[36:39], v[12:15]\n\t" \
    "v_mul_f32 v21, v21, v87\n\t" \
    "global_load_dwordx4 v[112:115], v91, s[56:57] offset:0 sc1\n\t" \
    "s_waitcnt lgkmcnt(5)\n\t" \
    "v_mfma_f32_16x16x32_bf16 v[0:3], v[136:139], v[40:43], v[0:3]\n\t" \
    "v_cvt_pk_bf16_f32 v82, v20, v21\n\t" \
    "ds_read_b128 v[32:35], v89 offset:16384\n\t" \
    "v_mfma_f32_16x16x32_bf16 v[4:7], v[140:143], v[40:43], v[4:7]\n\t" \
    "v_mul_f32 v86, v18, v18\n\t" \
    "s_waitcnt lgkmcnt(5)\n\t" \
    "v_mfma_f32_16x16x32_bf16 v[8:11], v[136:139], v[44:47], v[8:11]\n\t" \
    "v_mul_f32 v87, v19, v19\n\t" \
    "ds_read_b128 v[36:39], v89 offset:16640\n\t" \
    "v_mfma_f32_16x16x32_bf16 v[12:15], v[140:143], v[44:47], v[12:15]\n\t" \
    "v_fma_f32 v86, v86, v65, v64\n\t" \
    "global_load_dwordx4 v[116:119], v91, s[56:57] offset:1024 sc1\n\t" \
    "s_waitcnt lgkmcnt(5)\n\t" \
    "v_mfma_f32_16x16x32_bf16 v[0:3], v[144:147], v[48:51], v[0:3]\n\t" \
    "v_fma_f32 v87, v87, v65, v64\n\t" \
    "ds_read_b128 v[40:43], v89 offset:18432\n\t" \
    "v_mfma_f32_16x16x32_bf16 v[4:7], v[148:151], v[48:51], v[4:7]\n\t" \
    "v_mul_f32 v86, v86, v18\n\t" \
    "global_load_dwordx4 v[120:123], v91, s[56:57] offset:2048 sc1\n\t" \
    "s_waitcnt lgkmcnt(5)\n\t" \
    "v_mfma_f32_16x16x32_bf16 v[8:11], v[144:147], v[52:55], v[8:11]\n\t" \
    "v_mul_f32 v87, v87, v19\n\t" \
    "ds_read_b128 v[44:47], v89 offset:18688\n\t" \
    "v_mfma_f32_16x16x32_bf16 v[12:15], v[148:151], v[52:55], v[12:15]\n\t" \
    "v_exp_f32 v86, v86\n\t" \
    "s_waitcnt lgkmcnt(5)\n\t" \
    "v_mfma_f32_16x16x32_bf16 v[0:3], v[152:155], v[56:59], v[0:3]\n\t" \
    "v_exp_f32 v87, v87\n\t" \
    "ds_read_b128 v[48:51], v89 offset:20480\n\t" \
    "v_mfma_f32_16x16x32_bf16 v[4:7], v[156:159], v[56:59], v[4:7]\n\t" \
    "v_mul_f32 v22, v18, v22\n\t" \
    "global_load_dwordx4 v[124:127], v91, s[56:57] offset:3072 sc1\n\t" \
    "s_add_u32 s56, s56, 8192\n\t" \
    "s_addc_u32 s57, s57, 0\n\t" \
    "s_waitcnt lgkmcnt(5)\n\t" \
    "v_mfma_f32_16x16x32_bf16 v[8:11], v[152:155], v[60:63], v[8:11]\n\t" \
    "v_fma_f32 v86, v86, v66, v66\n\t" \
    "ds_read_b128 v[52:55], v89 offset:20736\n\t" \
    "v_mfma_f32_16x16x32_bf16 v[12:15], v[156:159], v[60:63], v[12:15]\n\t" \
    "v_fma_f32 v87, v87, v66, v66\n\t" \
    "global_load_dwordx4 v[128:131], v91, s[56:57] offset:-4096 sc1\n\t" \
    "s_waitcnt lgkmcnt(5)\n\t" \
    "v_mfma_f32_16x16x32_bf16 v[0:3], v[160:163], v[32:35], v[0:3]\n\t" \
    "v_rcp_f32 v86, v86\n\t" \
    "ds_read_b128 v[56:59], v89 offset:22528\n\t" \
    "v_mfma_f32_16x16x32_bf16 v[4:7], v[164:167], v[32:35], v[4:7]\n\t" \
    "v_rcp_f32 v87, v87\n\t" \
    "s_waitcnt lgkmcnt(5)\n\t" \
    "v_mfma_f32_16x16x32_bf16 v[8:11], v[160:163], v[36:39], v[8:11]\n\t" \
    "v_mul_f32 v23, v19, v23\n\t" \
    "ds_read_b128 v[60:63], v89 offset:22784\n\t" \
    "v_mfma_f32_16x16x32_bf16 v[12:15], v[164:167], v[36:39], v[12:15]\n\t" \
    "v_mul_f32 v22, v22, v86\n\t" \
    "global_load_dwordx4 v[132:135], v91, s[56:57] offset:-3072 sc1\n\t" \
    "s_waitcnt lgkmcnt(5)\n\t" \
    "v_mfma_f32_16x16x32_bf16 v[0:3], v[168:171], v[40:43], v[0:3]\n\t" \
    "v_mul_f32 v23, v23, v87\n\t" \
    "ds_read_b128 v[32:35], v89 offset:24576\n\t" \
    "v_mfma_f32_16x16x32_bf16 v[4:7], v[172:175], v[40:43], v[4:7]\n\t" \
    "v_cvt_pk_bf16_f32 v83, v22, v23\n\t" \
    "global_load_dwordx4 v[136:139], v91, s[56:57] offset:-2048 sc1\n\t" \
    "s_waitcnt lgkmcnt(5)\n\t" \
    "v_mfma_f32_16x16x32_bf16 v[8:11], v[168:171], v[44:47], v[8:11]\n\t" \
    "v_mul_f32 v86, v24, v24\n\t" \
    "ds_read_b128 v[36:39], v89 offset:24832\n\t" \
    "v_mfma_f32_16x16x32_bf16 v[12:15], v[172:175], v[44:47], v[12:15]\n\t" \
    "v_mul_f32 v87, v25, v25\n\t" \
    "s_waitcnt lgkmcnt(5)\n\t" \
    "v_mfma_f32_16x16x32_bf16 v[0:3], v[176:179], v[48:51], v[0:3]\n\t" \
    "v_fma_f32 v86, v86, v69, v68\n\t" \
    "ds_read_b128 v[40:43], v89 offset:26624\n\t" \
    "v_mfma_f32_16x16x32_bf16 v[4:7], v[180:183], v[48:51], v[4:7]\n\t" \
    "v_fma_f32 v87, v87, v69, v68\n\t" \
    "s_waitcnt vmcnt(11)\n\t" \
    "s_barrier\n\t" \
    "global_load_dwordx4 v[140:143], v91, s[56:57] offset:-1024 sc1\n\t" \
    "s_waitcnt lgkmcnt(5)\n\t" \
    "v_mfma_f32_16x16x32_bf16 v[8:11], v[176:179], v[52:55], v[8:11]\n\t" \
    "v_mul_f32 v86, v86, v24\n\t" \
    "ds_read_b128 v[44:47], v89 offset:26880\n\t" \
    "v_mfma_f32_16x16x32_bf16 v[12:15], v[180:183], v[52:55], v[12:15]\n\t" \
    "v_mul_f32 v87, v87, v25\n\t" \
    "s_add_u32 m0, s43, 83968\n\t" \
    "s_add_u32 s48, s54, 43008\n\t" \
    "s_addc_u32 s49, s55, 0\n\t" \
    "global_load_lds_dwordx4 v91, s[48:49] offset:-2048 sc1\n\t" \
    "s_waitcnt lgkmcnt(5)\n\t" \
    "v_mfma_f32_16x16x32_bf16 v[0:3], v[184:187], v[56:59], v[0:3]\n\t" \
    "v_exp_f32 v86, v86\n\t" \
    "ds_read_b128 v[48:51], v89 offset:28672\n\t" \
    "v_mfma_f32_16x16x32_bf16 v[4:7], v[188:191], v[56:59], v[4:7]\n\t" \
    "v_exp_f32 v87, v87\n\t" \
    "global_load_dwordx4 v[144:147], v91, s[56:57] offset:0 sc1\n\t" \
    "ds_read_b64 v[72:73], v93 offset:3088\n\t" \
    "ds_read_b64 v[74:75], v93 offset:3600\n\t" \
    "s_waitcnt lgkmcnt(7)\n\t" \
    "v_mfma_f32_16x16x32_bf16 v[8:11], v[184:187], v[60:63], v[8:11]\n\t" \
    "v_mul_f32 v28, v24, v28\n\t" \
    "ds_read_b128 v[52:55], v89 offset:28928\n\t" \
    "v_mfma_f32_16x16x32_bf16 v[12:15], v[188:191], v[60:63], v[12:15]\n\t" \
    "v_fma_f32 v86, v86, v70, v70\n\t" \
    "global_load_lds_dwordx4 v91, s[48:49] offset:-1024 sc1\n\t" \
    "s_waitcnt lgkmcnt(7)\n\t" \
    "v_mfma_f32_16x16x32_bf16 v[0:3], v[192:195], v[32:35], v[0:3]\n\t" \
    "v_fma_f32 v87, v87, v70, v70\n\t" \
    "ds_read_b128 v[56:59], v89 offset:30720\n\t" \
    "v_mfma_f32_16x16x32_bf16 v[4:7], v[196:199], v[32:35], v[4:7]\n\t" \
    "v_rcp_f32 v86, v86\n\t" \
    "global_load_dwordx4 v[148:151], v91, s[56:57] offset:1024 sc1\n\t" \
    "s_waitcnt lgkmcnt(7)\n\t" \
    "v_mfma_f32_16x16x32_bf16 v[8:11], v[192:195], v[36:39], v[8:11]\n\t" \
    "v_rcp_f32 v87, v87\n\t" \
    "ds_read_b128 v[60:63], v89 offset:30976\n\t" \
    "v_mfma_f32_16x16x32_bf16 v[12:15], v[196:199], v[36:39], v[12:15]\n\t" \
    "v_mul_f32 v29, v25, v29\n\t" \
    "global_load_lds_dwordx4 v91, s[48:49] offset:0 sc1\n\t" \
    "s_waitcnt lgkmcnt(7)\n\t" \
    "v_mfma_f32_16x16x32_bf16 v[0:3], v[200:203], v[40:43], v[0:3]\n\t" \
    "v_mul_f32 v28, v28, v86\n\t" \
    "ds_read_b128 v[32:35], v89 offset:32768\n\t" \
    "v_mfma_f32_16x16x32_bf16 v[4:7], v[204:207], v[40:43], v[4:7]\n\t" \
    "v_mul_f32 v29, v29, v87\n\t" \
    "global_load_dwordx4 v[152:155], v91, s[56:57] offset:2048 sc1\n\t" \
    "s_waitcnt lgkmcnt(7)\n\t" \
    "v_mfma_f32_16x16x32_bf16 v[8:11], v[200:203], v[44:47], v[8:11]\n\t" \
    "v_cvt_pk_bf16_f32 v84, v28, v29\n\t" \
    "ds_read_b128 v[36:39], v89 offset:33024\n\t" \
    "v_mfma_f32_16x16x32_bf16 v[12:15], v[204:207], v[44:47], v[12:15]\n\t" \
    "v_mul_f32 v86, v26, v26\n\t" \
    "global_load_lds_dwordx4 v91, s[48:49] offset:1024 sc1\n\t" \
    "s_waitcnt lgkmcnt(7)\n\t" \
    "v_mfma_f32_16x16x32_bf16 v[0:3], v[208:211], v[48:51], v[0:3]\n\t" \
    "v_mul_f32 v87, v27, v27\n\t" \
    "ds_read_b128 v[40:43], v89 offset:34816\n\t" \
    "v_mfma_f32_16x16x32_bf16 v[4:7], v[212:215], v[48:51], v[4:7]\n\t" \
    "v_fma_f32 v86, v86, v69, v68\n\t" \
    "global_load_dwordx4 v[156:159], v91, s[56:57] offset:3072 sc1\n\t" \
    "s_add_u32 s56, s56, 8192\n\t" \
    "s_addc_u32 s57, s57, 0\n\t" \
    "s_waitcnt lgkmcnt(5)\n\t" \
    "v_mfma_f32_16x16x32_bf16 v[8:11], v[208:211], v[52:55], v[8:11]\n\t" \
    "v_fma_f32 v87, v87, v69, v68\n\t" \
    "ds_read_b128 v[44:47], v89 offset:35072\n\t" \
    "v_mfma_f32_16x16x32_bf16 v[12:15], v[212:215], v[52:55], v[12:15]\n\t" \
    "v_mul_f32 v86, v86, v26\n\t" \
    "global_load_lds_dwordx4 v91, s[48:49] offset:2048 sc1\n\t" \
    "s_waitcnt lgkmcnt(5)\n\t" \
    "v_mfma_f32_16x16x32_bf16 v[0:3], v[216:219], v[56:59], v[0:3]\n\t" \
    "v_mul_f32 v87, v87, v27\n\t" \
    "ds_read_b128 v[48:51], v89 offset:36864\n\t" \
    "v_mfma_f32_16x16x32_bf16 v[4:7], v[220:223], v[56:59], v[4:7]\n\t" \
    "v_exp_f32 v86, v86\n\t" \
    "global_load_dwordx4 v[160:163], v91, s[56:57] offset:-4096 sc1\n\t" \
    "s_waitcnt lgkmcnt(5)\n\t" \
    "v_mfma_f32_16x16x32_bf16 v[8:11], v[216:219], v[60:63], v[8:11]\n\t" \
    "v_exp_f32 v87, v87\n\t" \
    "ds_read_b128 v[52:55], v89 offset:37120\n\t" \
    "v_mfma_f32_16x16x32_bf16 v[12:15], v[220:223], v[60:63], v[12:15]\n\t" \
    "v_mul_f32 v30, v26, v30\n\t" \
    "global_load_dwordx4 v[164:167], v91, s[56:57] offset:-3072 sc1\n\t" \
    "s_waitcnt lgkmcnt(5)\n\t" \
    "v_mfma_f32_16x16x32_bf16 v[0:3], v[224:227], v[32:35], v[0:3]\n\t" \
    "v_fma_f32 v86, v86, v70, v70\n\t" \
    "ds_read_b128 v[56:59], v89 offset:38912\n\t" \
    "v_mfma_f32_16x16x32_bf16 v[4:7], v[228:231], v[32:35], v[4:7]\n\t" \
    "v_fma_f32 v87, v87, v70, v70\n\t" \
    "global_load_dwordx4 v[168:171], v91, s[56:57] offset:-2048 sc1\n\t" \
    "s_waitcnt lgkmcnt(5)\n\t" \
    "v_mfma_f32_16x16x32_bf16 v[8:11], v[224:227], v[36:39], v[8:11]\n\t" \
    "v_rcp_f32 v86, v86\n\t" \
    "ds_read_b128 v[60:63], v89 offset:39168\n\t" \
    "v_mfma_f32_16x16x32_bf16 v[12:15], v[228:231], v[36:39], v[12:15]\n\t" \
    "v_rcp_f32 v87, v87\n\t" \
    "global_load_dwordx4 v[172:175], v91, s[56:57] offset:-1024 sc1\n\t" \
    "s_waitcnt lgkmcnt(5)\n\t" \
    "v_mfma_f32_16x16x32_bf16 v[0:3], v[232:235], v[40:43], v[0:3]\n\t" \
    "v_mul_f32 v31, v27, v31\n\t" \
    "v_mfma_f32_16x16x32_bf16 v[4:7], v[236:239], v[40:43], v[4:7]\n\t" \
    "v_mul_f32 v30, v30, v86\n\t" \
    "s_waitcnt lgkmcnt(4)\n\t" \
    "v_mfma_f32_16x16x32_bf16 v[8:11], v[232:235], v[44:47], v[8:11]\n\t" \
    "v_mul_f32 v31, v31, v87\n\t" \
    "v_mfma_f32_16x16x32_bf16 v[12:15], v[236:239], v[44:47], v[12:15]\n\t" \
    "v_cvt_pk_bf16_f32 v85, v30, v31\n\t" \
    "s_waitcnt lgkmcnt(3)\n\t" \
    "v_mfma_f32_16x16x32_bf16 v[0:3], v[240:243], v[48:51], v[0:3]\n\t" \
    "v_mfma_f32_16x16x32_bf16 v[4:7], v[244:247], v[48:51], v[4:7]\n\t" \
    "s_waitcnt lgkmcnt(2)\n\t" \
    "v_mfma_f32_16x16x32_bf16 v[8:11], v[240:243], v[52:55], v[8:11]\n\t" \
    "v_mfma_f32_16x16x32_bf16 v[12:15], v[244:247], v[52:55], v[12:15]\n\t" \
    "v_permlane16_swap_b32 v82, v84\n\t" \
    "v_permlane16_swap_b32 v83, v85\n\t" \
    "ds_read_b96 v[64:66], v93 offset:3072\n\t" \
    "ds_read_b96 v[68:70], v93 offset:3584\n\t" \
    "s_waitcnt lgkmcnt(3)\n\t" \
    "v_mfma_f32_16x16x32_bf16 v[0:3], v[248:251], v[56:59], v[0:3]\n\t" \
    "v_mfma_f32_16x16x32_bf16 v[4:7], v[252:255], v[56:59], v[4:7]\n\t" \
    "global_store_dwordx4 v92, v[82:85], s[44:45] sc1\n\t" \
    "s_add_u32 s44, s44, 110592\n\t" \
    "s_addc_u32 s45, s45, 0\n\t" \
    "s_waitcnt lgkmcnt(2)\n\t" \
    "v_mfma_f32_16x16x32_bf16 v[8:11], v[248:251], v[60:63], v[8:11]\n\t" \
    "v_mfma_f32_16x16x32_bf16 v[12:15], v[252:255], v[60:63], v[12:15]\n\t" \
    "v_mfma_f32_16x16x16_bf16 v[16:19], v[78:79], v[72:73], v[0:3]\n\t" \
    "v_mfma_f32_16x16x16_bf16 v[20:23], v[80:81], v[72:73], v[4:7]\n\t" \
    "v_mfma_f32_16x16x16_bf16 v[24:27], v[78:79], v[74:75], v[8:11]\n\t" \
    "v_mfma_f32_16x16x16_bf16 v[28:31], v[80:81], v[74:75], v[12:15]\n\t" \
    "s_mov_b32 m0, s47\n\t" \
    "s_waitcnt lgkmcnt(0)\n\t" \
    "s_nop 5\n\t" \
    "v_mul_f32 v0, v16, v16\n\t" \
    "v_mul_f32 v1, v17, v17\n\t" \
    "v_mul_f32 v2, v18, v18\n\t" \
    "v_mul_f32 v3, v19, v19\n\t" \
    "v_mul_f32 v4, v24, v24\n\t" \
    "v_mul_f32 v5, v25, v25\n\t" \
    "v_mul_f32 v6, v26, v26\n\t" \
    "v_mul_f32 v7, v27, v27\n\t" \
    "v_fma_f32 v0, v0, v65, v64\n\t" \
    "v_fma_f32 v1, v1, v65, v64\n\t" \
    "v_fma_f32 v2, v2, v65, v64\n\t" \
    "v_fma_f32 v3, v3, v65, v64\n\t" \
    "v_fma_f32 v4, v4, v69, v68\n\t" \
    "v_fma_f32 v5, v5, v69, v68\n\t" \
    "v_fma_f32 v6, v6, v69, v68\n\t" \
    "v_fma_f32 v7, v7, v69, v68\n\t" \
    "v_mul_f32 v0, v0, v16\n\t" \
    "v_mul_f32 v1, v1, v17\n\t" \
    "v_mul_f32 v2, v2, v18\n\t" \
    "v_mul_f32 v3, v3, v19\n\t" \
    "v_mul_f32 v4, v4, v24\n\t" \
    "v_mul_f32 v5, v5, v25\n\t" \
    "v_mul_f32 v6, v6, v26\n\t" \
    "v_mul_f32 v7, v7, v27\n\t" \
    "v_exp_f32 v0, v0\n\t" \
    "v_exp_f32 v1, v1\n\t" \
    "v_exp_f32 v2, v2\n\t" \
    "v_exp_f32 v3, v3\n\t" \
    "v_exp_f32 v4, v4\n\t" \
    "v_exp_f32 v5, v5\n\t" \
    "v_exp_f32 v6, v6\n\t" \
    "v_exp_f32 v7, v7\n\t" \
    "v_fma_f32 v0, v0, v66, v66\n\t" \
    "v_fma_f32 v1, v1, v66, v66\n\t" \
    "v_fma_f32 v2, v2, v66, v66\n\t" \
    "v_fma_f32 v3, v3, v66, v66\n\t" \
    "v_fma_f32 v4, v4, v70, v70\n\t" \
    "v_fma_f32 v5, v5, v70, v70\n\t" \
    "v_fma_f32 v6, v6, v70, v70\n\t" \
    "v_fma_f32 v7, v7, v70, v70\n\t" \
    "v_rcp_f32 v0, v0\n\t" \
    "v_mul_f32 v8, v16, v20\n\t" \
    "v_rcp_f32 v1, v1\n\t" \
    "v_mul_f32 v9, v17, v21\n\t" \
    "v_rcp_f32 v2, v2\n\t" \
    "v_mul_f32 v10, v18, v22\n\t" \
    "v_rcp_f32 v3, v3\n\t" \
    "v_mul_f32 v11, v19, v23\n\t" \
    "v_rcp_f32 v4, v4\n\t" \
    "v_mul_f32 v12, v24, v28\n\t" \
    "v_rcp_f32 v5, v5\n\t" \
    "v_mul_f32 v13, v25, v29\n\t" \
    "v_rcp_f32 v6, v6\n\t" \
    "v_mul_f32 v14, v26, v30\n\t" \
    "v_rcp_f32 v7, v7\n\t" \
    "v_mul_f32 v15, v27, v31\n\t" \
    "v_mul_f32 v0, v0, v8\n\t" \
    "v_mul_f32 v1, v1, v9\n\t" \
    "v_mul_f32 v2, v2, v10\n\t" \
    "v_mul_f32 v3, v3, v11\n\t" \
    "v_mul_f32 v4, v4, v12\n\t" \
    "v_mul_f32 v5, v5, v13\n\t" \
    "v_mul_f32 v6, v6, v14\n\t" \
    "v_mul_f32 v7, v7, v15\n\t" \
    "v_cvt_pk_bf16_f32 v82, v0, v1\n\t" \
    "v_cvt_pk_bf16_f32 v83, v2, v3\n\t" \
    "v_cvt_pk_bf16_f32 v84, v4, v5\n\t" \
    "v_cvt_pk_bf16_f32 v85, v6, v7\n\t" \
    "s_nop 1\n\t" \
    "v_permlane16_swap_b32 v82, v84\n\t" \
    "v_permlane16_swap_b32 v83, v85\n\t" \
    "global_store_dwordx4 v92, v[82:85], s[44:45] sc1\n\t" \
    "s_nop 1\n\t" \
    ""
#define OUT_SC ""
#define DOWN_ASM \
    "s_mov_b32 s47, 0x4000000\n\t" \
    "ds_read_b32 v251, v95\n\t" \
    "s_waitcnt lgkmcnt(0)\n\t" \
    "v_cmp_ne_u32 vcc, s54, v251\n\t" \
    "s_cbranch_vccz 50f\n\t" \
    "global_load_dwordx4 v[40:43], v89, s[44:45] offset:-4096 sc1\n\t" \
    "global_load_dwordx4 v[44:47], v89, s[44:45] offset:-3840 sc1\n\t" \
    "global_load_dwordx4 v[48:51], v89, s[44:45] offset:-2048 sc1\n\t" \
    "global_load_dwordx4 v[52:55], v89, s[44:45] offset:-1792 sc1\n\t" \
    "global_load_dwordx4 v[56:59], v89, s[44:45] offset:0 sc1\n\t" \
    "global_load_dwordx4 v[60:63], v89, s[44:45] offset:256 sc1\n\t" \
    "s_waitcnt vmcnt(6)\n\t" \
    "s_barrier\n\t" \
    "51:\n\t" \
    "global_load_dwordx4 v[64:67], v89, s[44:45] offset:2048 sc1\n\t" \
    "global_load_dwordx4 v[68:71], v89, s[44:45] offset:2304 sc1\n\t" \
    "global_load_dwordx4 v[72:75], v89, s[42:43] offset:-2048 sc1\n\t" \
    "global_load_dwordx4 v[76:79], v89, s[42:43] offset:-1792 sc1\n\t" \
    "ds_read_b128 v[176:179], v91 offset:0\n\t" \
    "ds_read_b128 v[180:183], v91 offset:1024\n\t" \
    "ds_read_b128 v[184:187], v91 offset:2048\n\t" \
    "ds_read_b128 v[188:191], v91 offset:3072\n\t" \
    "ds_read_b128 v[192:195], v91 offset:4096\n\t" \
    "ds_read_b128 v[196:199], v91 offset:5120\n\t" \
    "ds_read_b128 v[200:203], v91 offset:6144\n\t" \
    "ds_read_b128 v[204:207], v91 offset:7168\n\t" \
    "ds_read_b128 v[208:211], v91 offset:8192\n\t" \
    "ds_read_b128 v[212:215], v91 offset:9216\n\t" \
    "ds_read_b128 v[216:219], v91 offset:10240\n\t" \
    "ds_read_b128 v[220:223], v91 offset:11264\n\t" \
    "ds_read_b128 v[224:227], v91 offset:12288\n\t" \
    "ds_read_b128 v[228:231], v91 offset:13312\n\t" \
    "ds_read_b128 v[232:235], v91 offset:14336\n\t" \
    "s_waitcnt vmcnt(8)\n\t" \
    "v_mfma_f32_16x16x32_bf16 v[0:3], v[96:99], v[40:43], 0\n\t" \
    "v_mfma_f32_16x16x32_bf16 v[4:7], v[100:103], v[40:43], 0\n\t" \
    "v_mfma_f32_16x16x32_bf16 v[8:11], v[104:107], v[40:43], 0\n\t" \
    "v_mfma_f32_16x16x32_bf16 v[12:15], v[108:111], v[40:43], 0\n\t" \
    "v_mfma_f32_16x16x32_bf16 v[16:19], v[112:115], v[40:43], 0\n\t" \
    "v_mfma_f32_16x16x32_bf16 v[20:23], v[96:99], v[44:47], 0\n\t" \
    "v_mfma_f32_16x16x32_bf16 v[24:27], v[100:103], v[44:47], 0\n\t" \
    "v_mfma_f32_16x16x32_bf16 v[28:31], v[104:107], v[44:47], 0\n\t" \
    "v_mfma_f32_16x16x32_bf16 v[32:35], v[108:111], v[44:47], 0\n\t" \
    "v_mfma_f32_16x16x32_bf16 v[36:39], v[112:115], v[44:47], 0\n\t" \
    "global_load_dwordx4 v[80:83], v89, s[42:43] offset:0 sc1\n\t" \
    "global_load_dwordx4 v[84:87], v89, s[42:43] offset:256 sc1\n\t" \
    "s_waitcnt vmcnt(8)\n\t" \
    "v_mfma_f32_16x16x32_bf16 v[0:3], v[116:119], v[48:51], v[0:3]\n\t" \
    "v_mfma_f32_16x16x32_bf16 v[4:7], v[120:123], v[48:51], v[4:7]\n\t" \
    "v_mfma_f32_16x16x32_bf16 v[8:11], v[124:127], v[48:51], v[8:11]\n\t" \
    "v_mfma_f32_16x16x32_bf16 v[12:15], v[128:131], v[48:51], v[12:15]\n\t" \
    "v_mfma_f32_16x16x32_bf16 v[16:19], v[132:135], v[48:51], v[16:19]\n\t" \
    "v_mfma_f32_16x16x32_bf16 v[20:23], v[116:119], v[52:55], v[20:23]\n\t" \
    "v_mfma_f32_16x16x32_bf16 v[24:27], v[120:123], v[52:55], v[24:27]\n\t" \
    "v_mfma_f32_16x16x32_bf16 v[28:31], v[124:127], v[52:55], v[28:31]\n\t" \
    "v_mfma_f32_16x16x32_bf16 v[32:35], v[128:131], v[52:55], v[32:35]\n\t" \
    "v_mfma_f32_16x16x32_bf16 v[36:39], v[132:135], v[52:55], v[36:39]\n\t" \
    "global_load_dwordx4 v[236:239], v89, s[42:43] offset:2048 sc1\n\t" \
    "global_load_dwordx4 v[240:243], v89, s[42:43] offset:2304 sc1\n\t" \
    "s_waitcnt vmcnt(8)\n\t" \
    "v_mfma_f32_16x16x32_bf16 v[0:3], v[136:139], v[56:59], v[0:3]\n\t" \
    "v_mfma_f32_16x16x32_bf16 v[4:7], v[140:143], v[56:59], v[4:7]\n\t" \
    "v_mfma_f32_16x16x32_bf16 v[8:11], v[144:147], v[56:59], v[8:11]\n\t" \
    "v_mfma_f32_16x16x32_bf16 v[12:15], v[148:151], v[56:59], v[12:15]\n\t" \
    "v_mfma_f32_16x16x32_bf16 v[16:19], v[152:155], v[56:59], v[16:19]\n\t" \
    "v_mfma_f32_16x16x32_bf16 v[20:23], v[136:139], v[60:63], v[20:23]\n\t" \
    "v_mfma_f32_16x16x32_bf16 v[24:27], v[140:143], v[60:63], v[24:27]\n\t" \
    "v_mfma_f32_16x16x32_bf16 v[28:31], v[144:147], v[60:63], v[28:31]\n\t" \
    "v_mfma_f32_16x16x32_bf16 v[32:35], v[148:151], v[60:63], v[32:35]\n\t" \
    "v_mfma_f32_16x16x32_bf16 v[36:39], v[152:155], v[60:63], v[36:39]\n\t" \
    "s_waitcnt vmcnt(6)\n\t" \
    "v_mfma_f32_16x16x32_bf16 v[0:3], v[156:159], v[64:67], v[0:3]\n\t" \
    "v_mfma_f32_16x16x32_bf16 v[4:7], v[160:163], v[64:67], v[4:7]\n\t" \
    "v_mfma_f32_16x16x32_bf16 v[8:11], v[164:167], v[64:67], v[8:11]\n\t" \
    "v_mfma_f32_16x16x32_bf16 v[12:15], v[168:171], v[64:67], v[12:15]\n\t" \
    "v_mfma_f32_16x16x32_bf16 v[16:19], v[172:175], v[64:67], v[16:19]\n\t" \
    "v_mfma_f32_16x16x32_bf16 v[20:23], v[156:159], v[68:71], v[20:23]\n\t" \
    "v_mfma_f32_16x16x32_bf16 v[24:27], v[160:163], v[68:71], v[24:27]\n\t" \
    "v_mfma_f32_16x16x32_bf16 v[28:31], v[164:167], v[68:71], v[28:31]\n\t" \
    "v_mfma_f32_16x16x32_bf16 v[32:35], v[168:171], v[68:71], v[32:35]\n\t" \
    "v_mfma_f32_16x16x32_bf16 v[36:39], v[172:175], v[68:71], v[36:39]\n\t" \
    "s_waitcnt lgkmcnt(0)\n\t" \
    "s_waitcnt vmcnt(4)\n\t" \
    "v_mfma_f32_16x16x32_bf16 v[0:3], v[176:179], v[72:75], v[0:3]\n\t" \
    "v_mfma_f32_16x16x32_bf16 v[4:7], v[180:183], v[72:75], v[4:7]\n\t" \
    "v_mfma_f32_16x16x32_bf16 v[8:11], v[184:187], v[72:75], v[8:11]\n\t" \
    "v_mfma_f32_16x16x32_bf16 v[12:15], v[188:191], v[72:75], v[12:15]\n\t" \
    "v_mfma_f32_16x16x32_bf16 v[16:19], v[192:195], v[72:75], v[16:19]\n\t" \
    "v_mfma_f32_16x16x32_bf16 v[20:23], v[176:179], v[76:79], v[20:23]\n\t" \
    "v_mfma_f32_16x16x32_bf16 v[24:27], v[180:183], v[76:79], v[24:27]\n\t" \
    "v_mfma_f32_16x16x32_bf16 v[28:31], v[184:187], v[76:79], v[28:31]\n\t" \
    "v_mfma_f32_16x16x32_bf16 v[32:35], v[188:191], v[76:79], v[32:35]\n\t" \
    "v_mfma_f32_16x16x32_bf16 v[36:39], v[192:195], v[76:79], v[36:39]\n\t" \
    "s_waitcnt vmcnt(2)\n\t" \
    "v_mfma_f32_16x16x32_bf16 v[0:3], v[196:199], v[80:83], v[0:3]\n\t" \
    "v_mfma_f32_16x16x32_bf16 v[4:7], v[200:203], v[80:83], v[4:7]\n\t" \
    "v_mfma_f32_16x16x32_bf16 v[8:11], v[204:207], v[80:83], v[8:11]\n\t" \
    "v_mfma_f32_16x16x32_bf16 v[12:15], v[208:211], v[80:83], v[12:15]\n\t" \
    "v_mfma_f32_16x16x32_bf16 v[16:19], v[212:215], v[80:83], v[16:19]\n\t" \
    "v_mfma_f32_16x16x32_bf16 v[20:23], v[196:199], v[84:87], v[20:23]\n\t" \
    "v_mfma_f32_16x16x32_bf16 v[24:27], v[200:203], v[84:87], v[24:27]\n\t" \
    "v_mfma_f32_16x16x32_bf16 v[28:31], v[204:207], v[84:87], v[28:31]\n\t" \
    "v_mfma_f32_16x16x32_bf16 v[32:35], v[208:211], v[84:87], v[32:35]\n\t" \
    "v_mfma_f32_16x16x32_bf16 v[36:39], v[212:215], v[84:87], v[36:39]\n\t" \
    "s_waitcnt vmcnt(0)\n\t" \
    "v_mfma_f32_16x16x32_bf16 v[0:3], v[216:219], v[236:239], v[0:3]\n\t" \
    "v_mfma_f32_16x16x32_bf16 v[4:7], v[220:223], v[236:239], v[4:7]\n\t" \
    "v_mfma_f32_16x16x32_bf16 v[8:11], v[224:227], v[236:239], v[8:11]\n\t" \
    "v_mfma_f32_16x16x32_bf16 v[12:15], v[228:231], v[236:239], v[12:15]\n\t" \
    "v_mfma_f32_16x16x32_bf16 v[16:19], v[232:235], v[236:239], v[16:19]\n\t" \
    "v_mfma_f32_16x16x32_bf16 v[20:23], v[216:219], v[240:243], v[20:23]\n\t" \
    "v_mfma_f32_16x16x32_bf16 v[24:27], v[220:223], v[240:243], v[24:27]\n\t" \
    "v_mfma_f32_16x16x32_bf16 v[28:31], v[224:227], v[240:243], v[28:31]\n\t" \
    "v_mfma_f32_16x16x32_bf16 v[32:35], v[228:231], v[240:243], v[32:35]\n\t" \
    "v_mfma_f32_16x16x32_bf16 v[36:39], v[232:235], v[240:243], v[36:39]\n\t" \
    "22:\n\t" \
    "ds_read_b32 v251, v95 offset:4\n\t" \
    "s_waitcnt lgkmcnt(0)\n\t" \
    "v_cmp_ne_u32 vcc, s54, v251\n\t" \
    "s_cbranch_vccnz 23f\n\t" \
    "s_add_u32 s47, s47, -1\n\t" \
    "s_cbranch_scc1 22b\n\t" \
    "23:\n\t" \
    "global_load_dwordx4 v[244:247], v90, s[44:45] offset:-4096 sc1\n\t" \
    "global_load_dwordx4 v[248:251], v90, s[44:45] offset:-3840 sc1\n\t" \
    "global_load_dwordx4 v[40:43], v90, s[44:45] offset:-2048 sc1\n\t" \
    "global_load_dwordx4 v[44:47], v90, s[44:45] offset:-1792 sc1\n\t" \
    "global_load_dwordx4 v[48:51], v90, s[44:45] offset:0 sc1\n\t" \
    "global_load_dwordx4 v[52:55], v90, s[44:45] offset:256 sc1\n\t" \
    "global_load_dwordx4 v[56:59], v90, s[44:45] offset:2048 sc1\n\t" \
    "global_load_dwordx4 v[60:63], v90, s[44:45] offset:2304 sc1\n\t" \
    "s_nop 2\n\t" \
    "ds_write_b128 v91, v[0:3] offset:0\n\t" \
    "ds_write_b128 v91, v[4:7] offset:1024\n\t" \
    "ds_write_b128 v91, v[8:11] offset:2048\n\t" \
    "ds_write_b128 v91, v[12:15] offset:3072\n\t" \
    "ds_write_b128 v91, v[16:19] offset:4096\n\t" \
    "s_nop 1\n\t" \
    "ds_write_b128 v91, v[20:23] offset:5120\n\t" \
    "ds_write_b128 v91, v[24:27] offset:6144\n\t" \
    "ds_write_b128 v91, v[28:31] offset:7168\n\t" \
    "ds_write_b128 v91, v[32:35] offset:8192\n\t" \
    "ds_write_b128 v91, v[36:39] offset:9216\n\t" \
    "s_waitcnt lgkmcnt(0)\n\t" \
    "v_mov_b32 v236, 1\n\t" \
    "s_mov_b64 exec, 1\n\t" \
    "ds_add_u32 v95, v236 offset:8\n\t" \
    "s_mov_b64 exec, -1\n\t" \
    "s_waitcnt vmcnt(6)\n\t" \
    "v_mfma_f32_16x16x32_bf16 v[0:3], v[96:99], v[244:247], 0\n\t" \
    "v_mfma_f32_16x16x32_bf16 v[4:7], v[100:103], v[244:247], 0\n\t" \
    "v_mfma_f32_16x16x32_bf16 v[8:11], v[104:107], v[244:247], 0\n\t" \
    "v_mfma_f32_16x16x32_bf16 v[12:15], v[108:111], v[244:247], 0\n\t" \
    "v_mfma_f32_16x16x32_bf16 v[16:19], v[112:115], v[244:247], 0\n\t" \
    "v_mfma_f32_16x16x32_bf16 v[20:23], v[96:99], v[248:251], 0\n\t" \
    "v_mfma_f32_16x16x32_bf16 v[24:27], v[100:103], v[248:251], 0\n\t" \
    "v_mfma_f32_16x16x32_bf16 v[28:31], v[104:107], v[248:251], 0\n\t" \
    "v_mfma_f32_16x16x32_bf16 v[32:35], v[108:111], v[248:251], 0\n\t" \
    "v_mfma_f32_16x16x32_bf16 v[36:39], v[112:115], v[248:251], 0\n\t" \
    "global_load_dwordx4 v[64:67], v90, s[42:43] offset:-2048 sc1\n\t" \
    "global_load_dwordx4 v[68:71], v90, s[42:43] offset:-1792 sc1\n\t" \
    "s_waitcnt vmcnt(6)\n\t" \
    "v_mfma_f32_16x16x32_bf16 v[0:3], v[116:119], v[40:43], v[0:3]\n\t" \
    "v_mfma_f32_16x16x32_bf16 v[4:7], v[120:123], v[40:43], v[4:7]\n\t" \
    "v_mfma_f32_16x16x32_bf16 v[8:11], v[124:127], v[40:43], v[8:11]\n\t" \
    "v_mfma_f32_16x16x32_bf16 v[12:15], v[128:131], v[40:43], v[12:15]\n\t" \
    "v_mfma_f32_16x16x32_bf16 v[16:19], v[132:135], v[40:43], v[16:19]\n\t" \
    "v_mfma_f32_16x16x32_bf16 v[20:23], v[116:119], v[44:47], v[20:23]\n\t" \
    "v_mfma_f32_16x16x32_bf16 v[24:27], v[120:123], v[44:47], v[24:27]\n\t" \
    "v_mfma_f32_16x16x32_bf16 v[28:31], v[124:127], v[44:47], v[28:31]\n\t" \
    "v_mfma_f32_16x16x32_bf16 v[32:35], v[128:131], v[44:47], v[32:35]\n\t" \
    "v_mfma_f32_16x16x32_bf16 v[36:39], v[132:135], v[44:47], v[36:39]\n\t" \
    "global_load_dwordx4 v[72:75], v90, s[42:43] offset:0 sc1\n\t" \
    "global_load_dwordx4 v[76:79], v90, s[42:43] offset:256 sc1\n\t" \
    "s_waitcnt vmcnt(6)\n\t" \
    "v_mfma_f32_16x16x32_bf16 v[0:3], v[136:139], v[48:51], v[0:3]\n\t" \
    "v_mfma_f32_16x16x32_bf16 v[4:7], v[140:143], v[48:51], v[4:7]\n\t" \
    "v_mfma_f32_16x16x32_bf16 v[8:11], v[144:147], v[48:51], v[8:11]\n\t" \
    "v_mfma_f32_16x16x32_bf16 v[12:15], v[148:151], v[48:51], v[12:15]\n\t" \
    "v_mfma_f32_16x16x32_bf16 v[16:19], v[152:155], v[48:51], v[16:19]\n\t" \
    "v_mfma_f32_16x16x32_bf16 v[20:23], v[136:139], v[52:55], v[20:23]\n\t" \
    "v_mfma_f32_16x16x32_bf16 v[24:27], v[140:143], v[52:55], v[24:27]\n\t" \
    "v_mfma_f32_16x16x32_bf16 v[28:31], v[144:147], v[52:55], v[28:31]\n\t" \
    "v_mfma_f32_16x16x32_bf16 v[32:35], v[148:151], v[52:55], v[32:35]\n\t" \
    "v_mfma_f32_16x16x32_bf16 v[36:39], v[152:155], v[52:55], v[36:39]\n\t" \
    "global_load_dwordx4 v[80:83], v90, s[42:43] offset:2048 sc1\n\t" \
    "global_load_dwordx4 v[84:87], v90, s[42:43] offset:2304 sc1\n\t" \
    "s_waitcnt vmcnt(6)\n\t" \
    "v_mfma_f32_16x16x32_bf16 v[0:3], v[156:159], v[56:59], v[0:3]\n\t" \
    "v_mfma_f32_16x16x32_bf16 v[4:7], v[160:163], v[56:59], v[4:7]\n\t" \
    "v_mfma_f32_16x16x32_bf16 v[8:11], v[164:167], v[56:59], v[8:11]\n\t" \
    "v_mfma_f32_16x16x32_bf16 v[12:15], v[168:171], v[56:59], v[12:15]\n\t" \
    "v_mfma_f32_16x16x32_bf16 v[16:19], v[172:175], v[56:59], v[16:19]\n\t" \
    "v_mfma_f32_16x16x32_bf16 v[20:23], v[156:159], v[60:63], v[20:23]\n\t" \
    "v_mfma_f32_16x16x32_bf16 v[24:27], v[160:163], v[60:63], v[24:27]\n\t" \
    "v_mfma_f32_16x16x32_bf16 v[28:31], v[164:167], v[60:63], v[28:31]\n\t" \
    "v_mfma_f32_16x16x32_bf16 v[32:35], v[168:171], v[60:63], v[32:35]\n\t" \
    "v_mfma_f32_16x16x32_bf16 v[36:39], v[172:175], v[60:63], v[36:39]\n\t" \
    "global_load_dwordx4 v[252:255], v88, s[50:51] sc1\n\t" \
    "s_waitcnt vmcnt(5)\n\t" \
    "v_mfma_f32_16x16x32_bf16 v[0:3], v[176:179], v[64:67], v[0:3]\n\t" \
    "v_mfma_f32_16x16x32_bf16 v[4:7], v[180:183], v[64:67], v[4:7]\n\t" \
    "v_mfma_f32_16x16x32_bf16 v[8:11], v[184:187], v[64:67], v[8:11]\n\t" \
    "v_mfma_f32_16x16x32_bf16 v[12:15], v[188:191], v[64:67], v[12:15]\n\t" \
    "v_mfma_f32_16x16x32_bf16 v[16:19], v[192:195], v[64:67], v[16:19]\n\t" \
    "v_mfma_f32_16x16x32_bf16 v[20:23], v[176:179], v[68:71], v[20:23]\n\t" \
    "v_mfma_f32_16x16x32_bf16 v[24:27], v[180:183], v[68:71], v[24:27]\n\t" \
    "v_mfma_f32_16x16x32_bf16 v[28:31], v[184:187], v[68:71], v[28:31]\n\t" \
    "v_mfma_f32_16x16x32_bf16 v[32:35], v[188:191], v[68:71], v[32:35]\n\t" \
    "v_mfma_f32_16x16x32_bf16 v[36:39], v[192:195], v[68:71], v[36:39]\n\t" \
    "s_waitcnt vmcnt(3)\n\t" \
    "v_mfma_f32_16x16x32_bf16 v[0:3], v[196:199], v[72:75], v[0:3]\n\t" \
    "v_mfma_f32_16x16x32_bf16 v[4:7], v[200:203], v[72:75], v[4:7]\n\t" \
    "v_mfma_f32_16x16x32_bf16 v[8:11], v[204:207], v[72:75], v[8:11]\n\t" \
    "v_mfma_f32_16x16x32_bf16 v[12:15], v[208:211], v[72:75], v[12:15]\n\t" \
    "v_mfma_f32_16x16x32_bf16 v[16:19], v[212:215], v[72:75], v[16:19]\n\t" \
    "v_mfma_f32_16x16x32_bf16 v[20:23], v[196:199], v[76:79], v[20:23]\n\t" \
    "v_mfma_f32_16x16x32_bf16 v[24:27], v[200:203], v[76:79], v[24:27]\n\t" \
    "v_mfma_f32_16x16x32_bf16 v[28:31], v[204:207], v[76:79], v[28:31]\n\t" \
    "v_mfma_f32_16x16x32_bf16 v[32:35], v[208:211], v[76:79], v[32:35]\n\t" \
    "v_mfma_f32_16x16x32_bf16 v[36:39], v[212:215], v[76:79], v[36:39]\n\t" \
    "s_waitcnt vmcnt(1)\n\t" \
    "v_mfma_f32_16x16x32_bf16 v[0:3], v[216:219], v[80:83], v[0:3]\n\t" \
    "v_mfma_f32_16x16x32_bf16 v[4:7], v[220:223], v[80:83], v[4:7]\n\t" \
    "v_mfma_f32_16x16x32_bf16 v[8:11], v[224:227], v[80:83], v[8:11]\n\t" \
    "v_mfma_f32_16x16x32_bf16 v[12:15], v[228:231], v[80:83], v[12:15]\n\t" \
    "v_mfma_f32_16x16x32_bf16 v[16:19], v[232:235], v[80:83], v[16:19]\n\t" \
    "v_mfma_f32_16x16x32_bf16 v[20:23], v[216:219], v[84:87], v[20:23]\n\t" \
    "v_mfma_f32_16x16x32_bf16 v[24:27], v[220:223], v[84:87], v[24:27]\n\t" \
    "v_mfma_f32_16x16x32_bf16 v[28:31], v[224:227], v[84:87], v[28:31]\n\t" \
    "v_mfma_f32_16x16x32_bf16 v[32:35], v[228:231], v[84:87], v[32:35]\n\t" \
    "v_mfma_f32_16x16x32_bf16 v[36:39], v[232:235], v[84:87], v[36:39]\n\t" \
    "s_nop 2\n\t" \
    "ds_write_b128 v91, v[0:3] offset:10240\n\t" \
    "ds_write_b128 v91, v[4:7] offset:11264\n\t" \
    "ds_write_b128 v91, v[8:11] offset:12288\n\t" \
    "ds_write_b128 v91, v[12:15] offset:13312\n\t" \
    "ds_write_b128 v91, v[16:19] offset:14336\n\t" \
    "s_nop 1\n\t" \
    "ds_write_b128 v94, v[20:23] offset:0\n\t" \
    "ds_write_b128 v94, v[24:27] offset:1024\n\t" \
    "ds_write_b128 v94, v[28:31] offset:2048\n\t" \
    "ds_write_b128 v94, v[32:35] offset:3072\n\t" \
    "ds_write_b128 v94, v[36:39] offset:4096\n\t" \
    "s_waitcnt lgkmcnt(0)\n\t" \
    "s_waitcnt vmcnt(0)\n\t" \
    "s_cmp_lt_u32 s46, 3\n\t" \
    "s_cbranch_scc0 41f\n\t" \
    "global_load_dwordx4 v[80:83], v88, s[50:51] offset:3072 sc1\n\t" \
    "global_load_dwordx4 v[84:87], v88, s[52:53] sc1\n\t" \
    "46:\n\t" \
    "ds_read_b32 v251, v95 offset:8\n\t" \
    "s_waitcnt lgkmcnt(0)\n\t" \
    "v_subrev_u32 v251, s54, v251\n\t" \
    "v_cmp_lt_u32 vcc, 7, v251\n\t" \
    "s_cbranch_vccnz 47f\n\t" \
    "s_add_u32 s47, s47, -1\n\t" \
    "s_cbranch_scc1 46b\n\t" \
    "47:\n\t" \
    "v_add_u32 v64, 0xf000, v92\n\t" \
    "v_add_u32 v65, 0x1e000, v92\n\t" \
    "ds_read_b128 v[0:3], v65 offset:0\n\t" \
    "ds_read_b128 v[4:7], v65 offset:15360\n\t" \
    "ds_read_b128 v[8:11], v92 offset:40960\n\t" \
    "ds_read_b128 v[12:15], v92 offset:56320\n\t" \
    "ds_read_b128 v[16:19], v64 offset:10240\n\t" \
    "ds_read_b128 v[20:23], v64 offset:25600\n\t" \
    "ds_read_b128 v[24:27], v64 offset:40960\n\t" \
    "ds_read_b128 v[28:31], v92 offset:35840\n\t" \
    "s_waitcnt lgkmcnt(4)\n\t" \
    "ds_read_b128 v[32:35], v65 offset:5120\n\t" \
    "ds_read_b128 v[36:39], v65 offset:20480\n\t" \
    "ds_read_b128 v[40:43], v92 offset:46080\n\t" \
    "ds_read_b128 v[44:47], v64 offset:0\n\t" \
    "ds_read_b128 v[48:51], v64 offset:15360\n\t" \
    "ds_read_b128 v[52:55], v64 offset:30720\n\t" \
    "ds_read_b128 v[56:59], v64 offset:46080\n\t" \
    "ds_read_b128 v[60:63], v64 offset:56320\n\t" \
    "v_lshlrev_b32 v66, 16, v252\n\t" \
    "v_and_b32 v67, 0xffff0000, v252\n\t" \
    "v_lshlrev_b32 v68, 16, v253\n\t" \
    "v_and_b32 v69, 0xffff0000, v253\n\t" \
    "v_lshlrev_b32 v70, 16, v254\n\t" \
    "v_and_b32 v71, 0xffff0000, v254\n\t" \
    "v_lshlrev_b32 v72, 16, v255\n\t" \
    "v_and_b32 v73, 0xffff0000, v255\n\t" \
    "s_waitcnt lgkmcnt(8)\n\t" \
    "v_pk_add_f32 v[0:1], v[0:1], v[4:5]\n\t" \
    "v_pk_add_f32 v[2:3], v[2:3], v[6:7]\n\t" \
    "v_pk_add_f32 v[8:9], v[8:9], v[12:13]\n\t" \
    "v_pk_add_f32 v[10:11], v[10:11], v[14:15]\n\t" \
    "v_pk_add_f32 v[16:17], v[16:17], v[20:21]\n\t" \
    "v_pk_add_f32 v[18:19], v[18:19], v[22:23]\n\t" \
    "v_pk_add_f32 v[24:25], v[24:25], v[28:29]\n\t" \
    "v_pk_add_f32 v[26:27], v[26:27], v[30:31]\n\t" \
    "v_pk_add_f32 v[0:1], v[0:1], v[8:9]\n\t" \
    "v_pk_add_f32 v[2:3], v[2:3], v[10:11]\n\t" \
    "v_pk_add_f32 v[16:17], v[16:17], v[24:25]\n\t" \
    "v_pk_add_f32 v[18:19], v[18:19], v[26:27]\n\t" \
    "v_pk_add_f32 v[0:1], v[0:1], v[16:17]\n\t" \
    "v_pk_add_f32 v[2:3], v[2:3], v[18:19]\n\t" \
    "s_waitcnt lgkmcnt(0)\n\t" \
    "v_pk_add_f32 v[32:33], v[32:33], v[36:37]\n\t" \
    "v_pk_add_f32 v[34:35], v[34:35], v[38:39]\n\t" \
    "v_pk_add_f32 v[40:41], v[40:41], v[44:45]\n\t" \
    "v_pk_add_f32 v[42:43], v[42:43], v[46:47]\n\t" \
    "v_pk_add_f32 v[48:49], v[48:49], v[52:53]\n\t" \
    "v_pk_add_f32 v[50:51], v[50:51], v[54:55]\n\t" \
    "v_pk_add_f32 v[56:57], v[56:57], v[60:61]\n\t" \
    "v_pk_add_f32 v[58:59], v[58:59], v[62:63]\n\t" \
    "v_pk_add_f32 v[32:33], v[32:33], v[40:41]\n\t" \
    "v_pk_add_f32 v[34:35], v[34:35], v[42:43]\n\t" \
    "v_pk_add_f32 v[48:49], v[48:49], v[56:57]\n\t" \
    "v_pk_add_f32 v[50:51], v[50:51], v[58:59]\n\t" \
    "v_pk_add_f32 v[32:33], v[32:33], v[48:49]\n\t" \
    "v_pk_add_f32 v[34:35], v[34:35], v[50:51]\n\t" \
    "s_nop 1\n\t" \
    "v_permlane16_swap_b32 v0, v32\n\t" \
    "v_permlane16_swap_b32 v1, v33\n\t" \
    "v_permlane16_swap_b32 v2, v34\n\t" \
    "v_permlane16_swap_b32 v3, v35\n\t" \
    "s_nop 0\n\t" \
    "v_pk_add_f32 v[0:1], v[0:1], v[66:67]\n\t" \
    "v_pk_add_f32 v[2:3], v[2:3], v[68:69]\n\t" \
    "v_pk_add_f32 v[32:33], v[32:33], v[70:71]\n\t" \
    "v_pk_add_f32 v[34:35], v[34:35], v[72:73]\n\t" \
    "v_cvt_pk_bf16_f32 v76, v0, v1\n\t" \
    "v_cvt_pk_bf16_f32 v77, v2, v3\n\t" \
    "v_cvt_pk_bf16_f32 v78, v32, v33\n\t" \
    "v_cvt_pk_bf16_f32 v79, v34, v35\n\t" \
    "global_store_dwordx4 v93, v[76:79], s[48:49]" OUT_SC "\n\t" \
    "s_cmp_eq_u32 s46, 2\n\t" \
    "s_cbranch_scc1 42f\n\t" \
    "v_add_u32 v92, 0xc00, v92\n\t" \
    "v_add_u32 v93, 96, v93\n\t" \
    "s_waitcnt vmcnt(2)\n\t" \
    "v_mov_b64 v[252:253], v[80:81]\n\t" \
    "v_mov_b64 v[254:255], v[82:83]\n\t" \
    "s_barrier\n\t" \
    "v_add_u32 v64, 0xf000, v92\n\t" \
    "v_add_u32 v65, 0x1e000, v92\n\t" \
    "ds_read_b128 v[0:3], v65 offset:0\n\t" \
    "ds_read_b128 v[4:7], v65 offset:15360\n\t" \
    "ds_read_b128 v[8:11], v92 offset:40960\n\t" \
    "ds_read_b128 v[12:15], v92 offset:56320\n\t" \
    "ds_read_b128 v[16:19], v64 offset:10240\n\t" \
    "ds_read_b128 v[20:23], v64 offset:25600\n\t" \
    "ds_read_b128 v[24:27], v64 offset:40960\n\t" \
    "ds_read_b128 v[28:31], v92 offset:35840\n\t" \
    "s_waitcnt lgkmcnt(4)\n\t" \
    "ds_read_b128 v[32:35], v65 offset:5120\n\t" \
    "ds_read_b128 v[36:39], v65 offset:20480\n\t" \
    "ds_read_b128 v[40:43], v92 offset:46080\n\t" \
    "ds_read_b128 v[44:47], v64 offset:0\n\t" \
    "ds_read_b128 v[48:51], v64 offset:15360\n\t" \
    "ds_read_b128 v[52:55], v64 offset:30720\n\t" \
    "ds_read_b128 v[56:59], v64 offset:46080\n\t" \
    "ds_read_b128 v[60:63], v64 offset:56320\n\t" \
    "v_lshlrev_b32 v66, 16, v252\n\t" \
    "v_and_b32 v67, 0xffff0000, v252\n\t" \
    "v_lshlrev_b32 v68, 16, v253\n\t" \
    "v_and_b32 v69, 0xffff0000, v253\n\t" \
    "v_lshlrev_b32 v70, 16, v254\n\t" \
    "v_and_b32 v71, 0xffff0000, v254\n\t" \
    "v_lshlrev_b32 v72, 16, v255\n\t" \
    "v_and_b32 v73, 0xffff0000, v255\n\t" \
    "s_waitcnt lgkmcnt(8)\n\t" \
    "v_pk_add_f32 v[0:1], v[0:1], v[4:5]\n\t" \
    "v_pk_add_f32 v[2:3], v[2:3], v[6:7]\n\t" \
    "v_pk_add_f32 v[8:9], v[8:9], v[12:13]\n\t" \
    "v_pk_add_f32 v[10:11], v[10:11], v[14:15]\n\t" \
    "v_pk_add_f32 v[16:17], v[16:17], v[20:21]\n\t" \
    "v_pk_add_f32 v[18:19], v[18:19], v[22:23]\n\t" \
    "v_pk_add_f32 v[24:25], v[24:25], v[28:29]\n\t" \
    "v_pk_add_f32 v[26:27], v[26:27], v[30:31]\n\t" \
    "v_pk_add_f32 v[0:1], v[0:1], v[8:9]\n\t" \
    "v_pk_add_f32 v[2:3], v[2:3], v[10:11]\n\t" \
    "v_pk_add_f32 v[16:17], v[16:17], v[24:25]\n\t" \
    "v_pk_add_f32 v[18:19], v[18:19], v[26:27]\n\t" \
    "v_pk_add_f32 v[0:1], v[0:1], v[16:17]\n\t" \
    "v_pk_add_f32 v[2:3], v[2:3], v[18:19]\n\t" \
    "s_waitcnt lgkmcnt(0)\n\t" \
    "v_pk_add_f32 v[32:33], v[32:33], v[36:37]\n\t" \
    "v_pk_add_f32 v[34:35], v[34:35], v[38:39]\n\t" \
    "v_pk_add_f32 v[40:41], v[40:41], v[44:45]\n\t" \
    "v_pk_add_f32 v[42:43], v[42:43], v[46:47]\n\t" \
    "v_pk_add_f32 v[48:49], v[48:49], v[52:53]\n\t" \
    "v_pk_add_f32 v[50:51], v[50:51], v[54:55]\n\t" \
    "v_pk_add_f32 v[56:57], v[56:57], v[60:61]\n\t" \
    "v_pk_add_f32 v[58:59], v[58:59], v[62:63]\n\t" \
    "v_pk_add_f32 v[32:33], v[32:33], v[40:41]\n\t" \
    "v_pk_add_f32 v[34:35], v[34:35], v[42:43]\n\t" \
    "v_pk_add_f32 v[48:49], v[48:49], v[56:57]\n\t" \
    "v_pk_add_f32 v[50:51], v[50:51], v[58:59]\n\t" \
    "v_pk_add_f32 v[32:33], v[32:33], v[48:49]\n\t" \
    "v_pk_add_f32 v[34:35], v[34:35], v[50:51]\n\t" \
    "s_nop 1\n\t" \
    "v_permlane16_swap_b32 v0, v32\n\t" \
    "v_permlane16_swap_b32 v1, v33\n\t" \
    "v_permlane16_swap_b32 v2, v34\n\t" \
    "v_permlane16_swap_b32 v3, v35\n\t" \
    "s_nop 0\n\t" \
    "v_pk_add_f32 v[0:1], v[0:1], v[66:67]\n\t" \
    "v_pk_add_f32 v[2:3], v[2:3], v[68:69]\n\t" \
    "v_pk_add_f32 v[32:33], v[32:33], v[70:71]\n\t" \
    "v_pk_add_f32 v[34:35], v[34:35], v[72:73]\n\t" \
    "v_cvt_pk_bf16_f32 v76, v0, v1\n\t" \
    "v_cvt_pk_bf16_f32 v77, v2, v3\n\t" \
    "v_cvt_pk_bf16_f32 v78, v32, v33\n\t" \
    "v_cvt_pk_bf16_f32 v79, v34, v35\n\t" \
    "global_store_dwordx4 v93, v[76:79], s[48:49]" OUT_SC "\n\t" \
    "s_branch 43f\n\t" \
    "50:\n\t" \
    "s_waitcnt vmcnt(0)\n\t" \
    "s_barrier\n\t" \
    "20:\n\t" \
    "ds_read_b32 v251, v95 offset:0\n\t" \
    "s_waitcnt lgkmcnt(0)\n\t" \
    "v_cmp_ne_u32 vcc, s54, v251\n\t" \
    "s_cbranch_vccnz 21f\n\t" \
    "s_add_u32 s47, s47, -1\n\t" \
    "s_cbranch_scc1 20b\n\t" \
    "21:\n\t" \
    "global_load_dwordx4 v[40:43], v89, s[44:45] offset:-4096 sc1\n\t" \
    "global_load_dwordx4 v[44:47], v89, s[44:45] offset:-3840 sc1\n\t" \
    "global_load_dwordx4 v[48:51], v89, s[44:45] offset:-2048 sc1\n\t" \
    "global_load_dwordx4 v[52:55], v89, s[44:45] offset:-1792 sc1\n\t" \
    "global_load_dwordx4 v[56:59], v89, s[44:45] offset:0 sc1\n\t" \
    "global_load_dwordx4 v[60:63], v89, s[44:45] offset:256 sc1\n\t" \
    "s_branch 51b\n\t" \
    "42:\n\t" \
    "v_add_u32 v92, 0x800, v92\n\t" \
    "v_add_u32 v93, 0x14040, v93\n\t" \
    "s_waitcnt vmcnt(1)\n\t" \
    "v_mov_b64 v[252:253], v[84:85]\n\t" \
    "v_mov_b64 v[254:255], v[86:87]\n\t" \
    "41:\n\t" \
    "s_barrier\n\t" \
    "v_add_u32 v64, 0xf000, v92\n\t" \
    "v_add_u32 v65, 0x1e000, v92\n\t" \
    "ds_read_b128 v[0:3], v65 offset:10240\n\t" \
    "ds_read_b128 v[4:7], v65 offset:25600\n\t" \
    "ds_read_b128 v[8:11], v92 offset:51200\n\t" \
    "ds_read_b128 v[12:15], v64 offset:5120\n\t" \
    "ds_read_b128 v[16:19], v64 offset:20480\n\t" \
    "ds_read_b128 v[20:23], v64 offset:35840\n\t" \
    "ds_read_b128 v[24:27], v64 offset:51200\n\t" \
    "ds_read_b128 v[28:31], v65 offset:30720\n\t" \
    "s_waitcnt lgkmcnt(4)\n\t" \
    "ds_read_b128 v[32:35], v92 offset:0\n\t" \
    "ds_read_b128 v[36:39], v92 offset:5120\n\t" \
    "ds_read_b128 v[40:43], v92 offset:10240\n\t" \
    "ds_read_b128 v[44:47], v92 offset:15360\n\t" \
    "ds_read_b128 v[48:51], v92 offset:20480\n\t" \
    "ds_read_b128 v[52:55], v92 offset:25600\n\t" \
    "ds_read_b128 v[56:59], v92 offset:30720\n\t" \
    "ds_read_b128 v[60:63], v65 offset:35840\n\t" \
    "v_lshlrev_b32 v66, 16, v252\n\t" \
    "v_and_b32 v67, 0xffff0000, v252\n\t" \
    "v_lshlrev_b32 v68, 16, v253\n\t" \
    "v_and_b32 v69, 0xffff0000, v253\n\t" \
    "v_lshlrev_b32 v70, 16, v254\n\t" \
    "v_and_b32 v71, 0xffff0000, v254\n\t" \
    "v_lshlrev_b32 v72, 16, v255\n\t" \
    "v_and_b32 v73, 0xffff0000, v255\n\t" \
    "s_waitcnt lgkmcnt(8)\n\t" \
    "v_pk_add_f32 v[0:1], v[0:1], v[4:5]\n\t" \
    "v_pk_add_f32 v[2:3], v[2:3], v[6:7]\n\t" \
    "v_pk_add_f32 v[8:9], v[8:9], v[12:13]\n\t" \
    "v_pk_add_f32 v[10:11], v[10:11], v[14:15]\n\t" \
    "v_pk_add_f32 v[16:17], v[16:17], v[20:21]\n\t" \
    "v_pk_add_f32 v[18:19], v[18:19], v[22:23]\n\t" \
    "v_pk_add_f32 v[24:25], v[24:25], v[28:29]\n\t" \
    "v_pk_add_f32 v[26:27], v[26:27], v[30:31]\n\t" \
    "v_pk_add_f32 v[0:1], v[0:1], v[8:9]\n\t" \
    "v_pk_add_f32 v[2:3], v[2:3], v[10:11]\n\t" \
    "v_pk_add_f32 v[16:17], v[16:17], v[24:25]\n\t" \
    "v_pk_add_f32 v[18:19], v[18:19], v[26:27]\n\t" \
    "v_pk_add_f32 v[0:1], v[0:1], v[16:17]\n\t" \
    "v_pk_add_f32 v[2:3], v[2:3], v[18:19]\n\t" \
    "s_waitcnt lgkmcnt(0)\n\t" \
    "v_pk_add_f32 v[32:33], v[32:33], v[36:37]\n\t" \
    "v_pk_add_f32 v[34:35], v[34:35], v[38:39]\n\t" \
    "v_pk_add_f32 v[40:41], v[40:41], v[44:45]\n\t" \
    "v_pk_add_f32 v[42:43], v[42:43], v[46:47]\n\t" \
    "v_pk_add_f32 v[48:49], v[48:49], v[52:53]\n\t" \
    "v_pk_add_f32 v[50:51], v[50:51], v[54:55]\n\t" \
    "v_pk_add_f32 v[56:57], v[56:57], v[60:61]\n\t" \
    "v_pk_add_f32 v[58:59], v[58:59], v[62:63]\n\t" \
    "v_pk_add_f32 v[32:33], v[32:33], v[40:41]\n\t" \
    "v_pk_add_f32 v[34:35], v[34:35], v[42:43]\n\t" \
    "v_pk_add_f32 v[48:49], v[48:49], v[56:57]\n\t" \
    "v_pk_add_f32 v[50:51], v[50:51], v[58:59]\n\t" \
    "v_pk_add_f32 v[32:33], v[32:33], v[48:49]\n\t" \
    "v_pk_add_f32 v[34:35], v[34:35], v[50:51]\n\t" \
    "s_nop 1\n\t" \
    "v_permlane16_swap_b32 v0, v32\n\t" \
    "v_permlane16_swap_b32 v1, v33\n\t" \
    "v_permlane16_swap_b32 v2, v34\n\t" \
    "v_permlane16_swap_b32 v3, v35\n\t" \
    "s_nop 0\n\t" \
    "v_pk_add_f32 v[0:1], v[0:1], v[66:67]\n\t" \
    "v_pk_add_f32 v[2:3], v[2:3], v[68:69]\n\t" \
    "v_pk_add_f32 v[32:33], v[32:33], v[70:71]\n\t" \
    "v_pk_add_f32 v[34:35], v[34:35], v[72:73]\n\t" \
    "v_cvt_pk_bf16_f32 v76, v0, v1\n\t" \
    "v_cvt_pk_bf16_f32 v77, v2, v3\n\t" \
    "v_cvt_pk_bf16_f32 v78, v32, v33\n\t" \
    "v_cvt_pk_bf16_f32 v79, v34, v35\n\t" \
    "global_store_dwordx4 v93, v[76:79], s[48:49]" OUT_SC "\n\t" \
    "43:\n\t" \
    ""
#define VCL(a) "v" #a "0", "v" #a "1", "v" #a "2", "v" #a "3", "v" #a "4", "v" #a "5", "v" #a "6", "v" #a "7", "v" #a "8", "v" #a "9"
#define ATTN_CLOBBERS "v0", "v1", "v2", "v3", "v4", "v5", "v6", "v7", "v8", "v9", VCL(1), VCL(2), VCL(3), VCL(4), VCL(5), VCL(6), VCL(7), VCL(8), VCL(9), VCL(10), VCL(11), VCL(12), VCL(13), VCL(14), VCL(15), \
    "v160", "v161", "v162", "v163", "v172", "v173", "v176", "v177", "v178", "v179", VCL(18), VCL(19), VCL(20), VCL(21), VCL(22), VCL(23), "s48", "s49", "s56", "s57", "vcc", "scc", "memory"
#define W_CLOBBERS "v96", "v97", "v98", "v99", VCL(10), VCL(11), VCL(12), VCL(13), VCL(14), VCL(15), VCL(16), VCL(17), VCL(18), VCL(19), VCL(20), VCL(21), VCL(22), VCL(23), VCL(24), "v250", "v251", "v252", "v253", "v254", "v255", "memory"
template <int I, int THROTTLE> DI void wload4(const char* src, unsigned voff) {
    asm volatile("global_load_dwordx4 v[%c2:%c2+3], %0, %1 sc1\n\tglobal_load_dwordx4 v[%c2+4:%c2+7], %0, %1 offset:1024 sc1\n\t"
                 "global_load_dwordx4 v[%c2+8:%c2+11], %0, %1 offset:2048 sc1\n\tglobal_load_dwordx4 v[%c2+12:%c2+15], %0, %1 offset:3072 sc1\n\ts_waitcnt vmcnt(%3)"
                 :: "v"(voff), "s"(src + I * 4096), "i"(96 + 16 * I), "i"(THROTTLE) : W_CLOBBERS);
}
#define W_INOUT "+{v[96:111]}"(wq[0]), "+{v[112:127]}"(wq[1]), "+{v[128:143]}"(wq[2]), "+{v[144:159]}"(wq[3]), "+{v[160:175]}"(wq[4]), "+{v[176:191]}"(wq[5]), "+{v[192:207]}"(wq[6]), "+{v[208:223]}"(wq[7]), "+{v[224:239]}"(wq[8]), "+{v[240:255]}"(wq[9])
#define COLSUM_LO "v_mov_b32 v94, 0\n\t" "v_mov_b32 v95, 0\n\t" "v_dot2c_f32_bf16 v94, 0x3f803f80, v96\n\t" "v_dot2c_f32_bf16 v95, 0x3f803f80, v100\n\t" \
    "v_dot2c_f32_bf16 v94, 0x3f803f80, v97\n\t" "v_dot2c_f32_bf16 v95, 0x3f803f80, v101\n\t" "v_dot2c_f32_bf16 v94, 0x3f803f80, v98\n\t" "v_dot2c_f32_bf16 v95, 0x3f803f80, v102\n\t" \
    "v_dot2c_f32_bf16 v94, 0x3f803f80, v99\n\t" "v_dot2c_f32_bf16 v95, 0x3f803f80, v103\n\t" "v_dot2c_f32_bf16 v94, 0x3f803f80, v104\n\t" "v_dot2c_f32_bf16 v95, 0x3f803f80, v108\n\t" \
    "v_dot2c_f32_bf16 v94, 0x3f803f80, v105\n\t" "v_dot2c_f32_bf16 v95, 0x3f803f80, v109\n\t" "v_dot2c_f32_bf16 v94, 0x3f803f80, v106\n\t" "v_dot2c_f32_bf16 v95, 0x3f803f80, v110\n\t" \
    "v_dot2c_f32_bf16 v94, 0x3f803f80, v107\n\t" "v_dot2c_f32_bf16 v95, 0x3f803f80, v111\n\t" "v_dot2c_f32_bf16 v94, 0x3f803f80, v112\n\t" "v_dot2c_f32_bf16 v95, 0x3f803f80, v116\n\t" \
    "v_dot2c_f32_bf16 v94, 0x3f803f80, v113\n\t" "v_dot2c_f32_bf16 v95, 0x3f803f80, v117\n\t" "v_dot2c_f32_bf16 v94, 0x3f803f80, v114\n\t" "v_dot2c_f32_bf16 v95, 0x3f803f80, v118\n\t" \
    "v_dot2c_f32_bf16 v94, 0x3f803f80, v115\n\t" "v_dot2c_f32_bf16 v95, 0x3f803f80, v119\n\t" "v_dot2c_f32_bf16 v94, 0x3f803f80, v120\n\t" "v_dot2c_f32_bf16 v95, 0x3f803f80, v124\n\t" \
    "v_dot2c_f32_bf16 v94, 0x3f803f80, v121\n\t" "v_dot2c_f32_bf16 v95, 0x3f803f80, v125\n\t" "v_dot2c_f32_bf16 v94, 0x3f803f80, v122\n\t" "v_dot2c_f32_bf16 v95, 0x3f803f80, v126\n\t" \
    "v_dot2c_f32_bf16 v94, 0x3f803f80, v123\n\t" "v_dot2c_f32_bf16 v95, 0x3f803f80, v127\n\t" "v_dot2c_f32_bf16 v94, 0x3f803f80, v128\n\t" "v_dot2c_f32_bf16 v95, 0x3f803f80, v132\n\t" \
    "v_dot2c_f32_bf16 v94, 0x3f803f80, v129\n\t" "v_dot2c_f32_bf16 v95, 0x3f803f80, v133\n\t" "v_dot2c_f32_bf16 v94, 0x3f803f80, v130\n\t" "v_dot2c_f32_bf16 v95, 0x3f803f80, v134\n\t" \
    "v_dot2c_f32_bf16 v94, 0x3f803f80, v131\n\t" "v_dot2c_f32_bf16 v95, 0x3f803f80, v135\n\t" "v_dot2c_f32_bf16 v94, 0x3f803f80, v136\n\t" "v_dot2c_f32_bf16 v95, 0x3f803f80, v140\n\t" \
    "v_dot2c_f32_bf16 v94, 0x3f803f80, v137\n\t" "v_dot2c_f32_bf16 v95, 0x3f803f80, v141\n\t" "v_dot2c_f32_bf16 v94, 0x3f803f80, v138\n\t" "v_dot2c_f32_bf16 v95, 0x3f803f80, v142\n\t" \
    "v_dot2c_f32_bf16 v94, 0x3f803f80, v139\n\t" "v_dot2c_f32_bf16 v95, 0x3f803f80, v143\n\t"
#define COLSUM_HI "v_dot2c_f32_bf16 v94, 0x3f803f80, v144\n\t" "v_dot2c_f32_bf16 v95, 0x3f803f80, v148\n\t" "v_dot2c_f32_bf16 v94, 0x3f803f80, v145\n\t" "v_dot2c_f32_bf16 v95, 0x3f803f80, v149\n\t" \
    "v_dot2c_f32_bf16 v94, 0x3f803f80, v146\n\t" "v_dot2c_f32_bf16 v95, 0x3f803f80, v150\n\t" "v_dot2c_f32_bf16 v94, 0x3f803f80, v147\n\t" "v_dot2c_f32_bf16 v95, 0x3f803f80, v151\n\t" \
    "v_dot2c_f32_bf16 v94, 0x3f803f80, v152\n\t" "v_dot2c_f32_bf16 v95, 0x3f803f80, v156\n\t" "v_dot2c_f32_bf16 v94, 0x3f803f80, v153\n\t" "v_dot2c_f32_bf16 v95, 0x3f803f80, v157\n\t" \
    "v_dot2c_f32_bf16 v94, 0x3f803f80, v154\n\t" "v_dot2c_f32_bf16 v95, 0x3f803f80, v158\n\t" "v_dot2c_f32_bf16 v94, 0x3f803f80, v155\n\t" "v_dot2c_f32_bf16 v95, 0x3f803f80, v159\n\t" \
    "v_dot2c_f32_bf16 v94, 0x3f803f80, v160\n\t" "v_dot2c_f32_bf16 v95, 0x3f803f80, v164\n\t" "v_dot2c_f32_bf16 v94, 0x3f803f80, v161\n\t" "v_dot2c_f32_bf16 v95, 0x3f803f80, v165\n\t" \
    "v_dot2c_f32_bf16 v94, 0x3f803f80, v162\n\t" "v_dot2c_f32_bf16 v95, 0x3f803f80, v166\n\t" "v_dot2c_f32_bf16 v94, 0x3f803f80, v163\n\t" "v_dot2c_f32_bf16 v95, 0x3f803f80, v167\n\t" \
    "v_dot2c_f32_bf16 v94, 0x3f803f80, v168\n\t" "v_dot2c_f32_bf16 v95, 0x3f803f80, v172\n\t" "v_dot2c_f32_bf16 v94, 0x3f803f80, v169\n\t" "v_dot2c_f32_bf16 v95, 0x3f803f80, v173\n\t" \
    "v_dot2c_f32_bf16 v94, 0x3f803f80, v170\n\t" "v_dot2c_f32_bf16 v95, 0x3f803f80, v174\n\t" "v_dot2c_f32_bf16 v94, 0x3f803f80, v171\n\t" "v_dot2c_f32_bf16 v95, 0x3f803f80, v175\n\t" \
    "v_dot2c_f32_bf16 v94, 0x3f803f80, v176\n\t" "v_dot2c_f32_bf16 v95, 0x3f803f80, v180\n\t" "v_dot2c_f32_bf16 v94, 0x3f803f80, v177\n\t" "v_dot2c_f32_bf16 v95, 0x3f803f80, v181\n\t" \
    "v_dot2c_f32_bf16 v94, 0x3f803f80, v178\n\t" "v_dot2c_f32_bf16 v95, 0x3f803f80, v182\n\t" "v_dot2c_f32_bf16 v94, 0x3f803f80, v179\n\t" "v_dot2c_f32_bf16 v95, 0x3f803f80, v183\n\t" \
    "v_dot2c_f32_bf16 v94, 0x3f803f80, v184\n\t" "v_dot2c_f32_bf16 v95, 0x3f803f80, v188\n\t" "v_dot2c_f32_bf16 v94, 0x3f803f80, v185\n\t" "v_dot2c_f32_bf16 v95, 0x3f803f80, v189\n\t" \
    "v_dot2c_f32_bf16 v94, 0x3f803f80, v186\n\t" "v_dot2c_f32_bf16 v95, 0x3f803f80, v190\n\t" "v_dot2c_f32_bf16 v94, 0x3f803f80, v187\n\t" "v_dot2c_f32_bf16 v95, 0x3f803f80, v191\n\t" \
    "v_dot2c_f32_bf16 v94, 0x3f803f80, v192\n\t" "v_dot2c_f32_bf16 v95, 0x3f803f80, v196\n\t" "v_dot2c_f32_bf16 v94, 0x3f803f80, v193\n\t" "v_dot2c_f32_bf16 v95, 0x3f803f80, v197\n\t" \
    "v_dot2c_f32_bf16 v94, 0x3f803f80, v194\n\t" "v_dot2c_f32_bf16 v95, 0x3f803f80, v198\n\t" "v_dot2c_f32_bf16 v94, 0x3f803f80, v195\n\t" "v_dot2c_f32_bf16 v95, 0x3f803f80, v199\n\t" \
    "v_dot2c_f32_bf16 v94, 0x3f803f80, v200\n\t" "v_dot2c_f32_bf16 v95, 0x3f803f80, v204\n\t" "v_dot2c_f32_bf16 v94, 0x3f803f80, v201\n\t" "v_dot2c_f32_bf16 v95, 0x3f803f80, v205\n\t" \
    "v_dot2c_f32_bf16 v94, 0x3f803f80, v202\n\t" "v_dot2c_f32_bf16 v95, 0x3f803f80, v206\n\t" "v_dot2c_f32_bf16 v94, 0x3f803f80, v203\n\t" "v_dot2c_f32_bf16 v95, 0x3f803f80, v207\n\t" \
    "v_dot2c_f32_bf16 v94, 0x3f803f80, v208\n\t" "v_dot2c_f32_bf16 v95, 0x3f803f80, v212\n\t" "v_dot2c_f32_bf16 v94, 0x3f803f80, v209\n\t" "v_dot2c_f32_bf16 v95, 0x3f803f80, v213\n\t" \
    "v_dot2c_f32_bf16 v94, 0x3f803f80, v210\n\t" "v_dot2c_f32_bf16 v95, 0x3f803f80, v214\n\t" "v_dot2c_f32_bf16 v94, 0x3f803f80, v211\n\t" "v_dot2c_f32_bf16 v95, 0x3f803f80, v215\n\t" \
    "v_dot2c_f32_bf16 v94, 0x3f803f80, v216\n\t" "v_dot2c_f32_bf16 v95, 0x3f803f80, v220\n\t" "v_dot2c_f32_bf16 v94, 0x3f803f80, v217\n\t" "v_dot2c_f32_bf16 v95, 0x3f803f80, v221\n\t" \
    "v_dot2c_f32_bf16 v94, 0x3f803f80, v218\n\t" "v_dot2c_f32_bf16 v95, 0x3f803f80, v222\n\t" "v_dot2c_f32_bf16 v94, 0x3f803f80, v219\n\t" "v_dot2c_f32_bf16 v95, 0x3f803f80, v223\n\t" \
    "v_dot2c_f32_bf16 v94, 0x3f803f80, v224\n\t" "v_dot2c_f32_bf16 v95, 0x3f803f80, v228\n\t" "v_dot2c_f32_bf16 v94, 0x3f803f80, v225\n\t" "v_dot2c_f32_bf16 v95, 0x3f803f80, v229\n\t" \
    "v_dot2c_f32_bf16 v94, 0x3f803f80, v226\n\t" "v_dot2c_f32_bf16 v95, 0x3f803f80, v230\n\t" "v_dot2c_f32_bf16 v94, 0x3f803f80, v227\n\t" "v_dot2c_f32_bf16 v95, 0x3f803f80, v231\n\t" \
    "v_dot2c_f32_bf16 v94, 0x3f803f80, v232\n\t" "v_dot2c_f32_bf16 v95, 0x3f803f80, v236\n\t" "v_dot2c_f32_bf16 v94, 0x3f803f80, v233\n\t" "v_dot2c_f32_bf16 v95, 0x3f803f80, v237\n\t" \
    "v_dot2c_f32_bf16 v94, 0x3f803f80, v234\n\t" "v_dot2c_f32_bf16 v95, 0x3f803f80, v238\n\t" "v_dot2c_f32_bf16 v94, 0x3f803f80, v235\n\t" "v_dot2c_f32_bf16 v95, 0x3f803f80, v239\n\t" \
    "v_dot2c_f32_bf16 v94, 0x3f803f80, v240\n\t" "v_dot2c_f32_bf16 v95, 0x3f803f80, v244\n\t" "v_dot2c_f32_bf16 v94, 0x3f803f80, v241\n\t" "v_dot2c_f32_bf16 v95, 0x3f803f80, v245\n\t" \
    "v_dot2c_f32_bf16 v94, 0x3f803f80, v242\n\t" "v_dot2c_f32_bf16 v95, 0x3f803f80, v246\n\t" "v_dot2c_f32_bf16 v94, 0x3f803f80, v243\n\t" "v_dot2c_f32_bf16 v95, 0x3f803f80, v247\n\t" \
    "v_dot2c_f32_bf16 v94, 0x3f803f80, v248\n\t" "v_dot2c_f32_bf16 v95, 0x3f803f80, v252\n\t" "v_dot2c_f32_bf16 v94, 0x3f803f80, v249\n\t" "v_dot2c_f32_bf16 v95, 0x3f803f80, v253\n\t" \
    "v_dot2c_f32_bf16 v94, 0x3f803f80, v250\n\t" "v_dot2c_f32_bf16 v95, 0x3f803f80, v254\n\t" "v_dot2c_f32_bf16 v94, 0x3f803f80, v251\n\t" "v_dot2c_f32_bf16 v95, 0x3f803f80, v255\n\t"
DI int lane_id() { unsigned z; asm volatile("s_mov_b32 %0, 0" : "=s"(z)); return (int)__builtin_amdgcn_mbcnt_hi(~0u, __builtin_amdgcn_mbcnt_lo(~0u, z)); }
DI int vrow(int r) { return r < 16 ? (r & 3) + (r & 4) * 2 + (r & 8) / 2 : r; }
DI void store_o(char* ws, int T, int head, int lane, const float* o, float inv) {
    const unsigned ob = WS_O + (unsigned)T * 40960;
    st16(ws, ob + head * 1024 + lane * 16, (u32x4){pk2(o[0] * inv, o[1] * inv), pk2(o[2] * inv, o[3] * inv), pk2(o[4] * inv, o[5] * inv), pk2(o[6] * inv, o[7] * inv)});
    if (lane < 32) st8(ws, ob + 32768 + head * 256 + lane * 8, (u32x2){pk2(o[8] * inv, o[9] * inv), pk2(o[10] * inv, o[11] * inv)});
}

DI void attn_exact(char* ws, int T, int head, int lane) {
    const int r32 = lane & 31, hf = lane >> 5, hp = head >> 1, odd = head & 1;
    const char* qb = ws + WS_Q + (unsigned)(hp * 64 + T) * TILE;
    const char* kb = ws + WS_K + (unsigned)(hp * 64) * TILE;
    const unsigned ko0 = odd * 1536 + lane * 16, ko1 = 1024 + r32 * 16 + odd * 8, vrel = (WS_V - WS_K) + hf * 640 + (odd * DH + min(vrow(r32), DH - 1)) * 16;
    u32x4 q0, k0, v0, v1; u32x2 q1, k1;
    gld16<0, true>(q0, qb, ko0); gld8<0, true>(q1, qb, ko1); wait_vm<0>(); PIN(q0); PIN(q1);
    if (hf) q1 = (u32x2){0u, 0u};
    f32x16 oo = {}; float m = -INFINITY;
#pragma unroll 1
    for (int t = 0; t < 64; ++t) {
        gld16<0, true>(k0, kb + t * TILE, ko0); gld8<0, true>(k1, kb + t * TILE, ko1); gld16<0, true>(v0, kb + t * TILE, vrel); gld16<1280, true>(v1, kb + t * TILE, vrel);
        wait_vm<0>(); PIN(k0); PIN(k1); PIN(v0); PIN(v1);
        if (r32 >= DH) { v0 = (u32x4){ONES, ONES, ONES, ONES}; v1 = v0; }
        const f32x16 z = {};
        f32x16 s = mfma32k8(k1, q1, mfma32(k0, q0, z));
        float mn = m;
#pragma unroll
        for (int i = 0; i < 16; ++i) mn = fmaxf(mn, s[i]);
        mn = fmaxf(mn, __shfl_xor(mn, 32));
        const float al = __builtin_amdgcn_exp2f(m - mn);
        m = mn;
        u32x4 p0, p1;
#pragma unroll
        for (int i = 0; i < 16; ++i) oo[i] *= al;
#pragma unroll
        for (int i = 0; i < 4; ++i) {
            p0[i] = pk2(__builtin_amdgcn_exp2f(s[2 * i] - mn), __builtin_amdgcn_exp2f(s[2 * i + 1] - mn));
            p1[i] = pk2(__builtin_amdgcn_exp2f(s[8 + 2 * i] - mn), __builtin_amdgcn_exp2f(s[9 + 2 * i] - mn));
        }
        oo = mfma32(v0, p0, oo); oo = mfma32(v1, p1, oo);
    }
    float o[12];
#pragma unroll
    for (int i = 0; i < 12; ++i) o[i] = oo[i];
    store_o(ws, T, head, lane, o, 1.f / oo[12]);
}

__global__ __launch_bounds__(512, 2) void block_kernel(const u16* __restrict__ x, const u16* __restrict__ wqkv, const u16* __restrict__ wo, const u16* __restrict__ wg,
                                                       const u16* __restrict__ wu, const u16* __restrict__ wd, u16* __restrict__ out, char* ws) {
    extern __shared__ __attribute__((aligned(16))) char smem[];
    const char* pc0; int code_bytes;
    asm volatile("s_getpc_b64 %0\n.Lcode_lo:\n\ts_mov_b32 %1, .Lcode_hi - .Lcode_lo" : "=s"(pc0), "=s"(code_bytes));
    LAS char* sm = (LAS char*)smem;
    const unsigned lds0 = (unsigned)(uintptr_t)sm;
    int lane = threadIdx.x & 63; const int wid = __builtin_amdgcn_readfirstlane(threadIdx.x >> 6);
    const int bid = blockIdx.x, xcd = bid & 7, idx = bid >> 3;
    const int head = xcd * 4 + (idx >> 3), qp = idx & 7;
    const int pm64 = xcd * 4 + (idx & 3), ct = idx >> 2;
    const int pm128 = (xcd >> 1) * 4 + (idx & 3), cj = (xcd & 1) * 8 + (idx >> 2), u0 = (27 * cj) >> 2;
    LAS volatile unsigned* ctl = (LAS volatile unsigned*)(sm + L_CTL);
    unsigned tag;
    if (bid < 16 && wid >= 4) glds_touch(pc0, (unsigned)max(min(((idx * 4 + wid - 4) * 64 + lane) * 128, code_bytes), 0), lds0);
    {
        const bool spare = idx < 2;
        const int j = (xcd & 1) * 30 + idx - 2, pn = spare ? 15 : j >> 2, pm = (xcd >> 1) * 4 + (spare ? (xcd & 1) * 2 + idx : j & 3);
        auto mk_tag = [](unsigned e0) { const unsigned e = __builtin_amdgcn_readfirstlane(e0), t = e * 0x9E3779B1u + 0x7F4A7C15u; return t == e ? t ^ 0x5bd1e995u : t; };
        if (wid < 4) {
            unsigned e0; u32x2 sx[5], sw[5];
            const int srow = pm * 128 + pn * 8 + wid * 2 + (lane >> 5);
            unsigned xd = WS_XS + (unsigned)srow * 16, cd = WS_CS + (unsigned)((pn * 16 + pm) * 1024 + wid * 256 + (lane & 7) * 32 + (lane & 8) * 2);
            auto side = [&](auto GI) {
                if constexpr (GI.v == -1) {
                    const unsigned o = (unsigned)(srow * (D * 2) + (lane & 31) * 8), o2 = (unsigned)((pm * 40 + (lane >> 3)) * (3 * D * 2) + min(pn, 14) * 256 + wid * 64 + (lane & 7) * 8);
                    gld4sc(e0, ws, 0u);
                    sfor<5>([&](auto I) { gld8<I.v * 256, false>(sx[I.v], x, o); });
                    sfor<5>([&](auto I) { gld8<0, false>(sw[I.v], (const char*)wqkv + I.v * 8 * (3 * D * 2), o2); });
                    PIN(xd); PIN(cd);
                }
                if constexpr (GI.v == 7) {
                    wait_vm<0>(); PIN(e0);
                    tag = mk_tag(e0);
                    float v[20], s1 = 0.f, s2 = 0.f, c4[4] = {0.f, 0.f, 0.f, 0.f};
#pragma unroll
                    for (int i = 0; i < 5; ++i) {
                        PIN(sx[i]); PIN(sw[i]);
                        v[4 * i] = bf_lo(sx[i][0]); v[4 * i + 1] = bf_hi(sx[i][0]); v[4 * i + 2] = bf_lo(sx[i][1]); v[4 * i + 3] = bf_hi(sx[i][1]);
                        s1 += (v[4 * i] + v[4 * i + 1]) + (v[4 * i + 2] + v[4 * i + 3]);
                        c4[0] += bf_lo(sw[i][0]); c4[1] += bf_hi(sw[i][0]); c4[2] += bf_lo(sw[i][1]); c4[3] += bf_hi(sw[i][1]);
                    }
                    const float mean = sum_x16(sum_row(s1)) * (1.f / D);
#pragma unroll
                    for (int i = 0; i < 20; ++i) s2 = fmaf(v[i] - mean, v[i] - mean, s2);
                    const float rstd = __builtin_amdgcn_rsqf(sum_x16(sum_row(s2)) * (1.f / D) + LN_EPS);
                    st16_if(ws, xd, (u32x4){__float_as_uint(rstd), tag, __float_as_uint(-mean), tag}, 0x100000001ull);
                    const float t0 = sum_x8(c4[0]), t1 = sum_x8(c4[1]), t2 = sum_x8(c4[2]), t3 = sum_x8(c4[3]);
                    const float ca = sum_x32(sum_x16((lane & 8) ? t2 : t0)), cb = sum_x32(sum_x16((lane & 8) ? t3 : t1));
                    if (!spare) st16_if(ws, cd, (u32x4){__float_as_uint(ca), tag, __float_as_uint(cb), tag}, 0xffffull);
                }
            };
            if (!spare) { if (pn >= 10) qkv_compute<true>(ws, sm, pm, pn, wid, lane, side); else qkv_compute<false>(ws, sm, pm, pn, wid, lane, side); }
            else { side(IC<-1>{}); bar(); side(IC<7>{}); bar(); }
        } else {
            const int hw = wid - 4;
            if (!spare) {
                unsigned xo[4], wo_[4];
                const char* xb = (const char*)x + pm * 128 * D * 2 - 4096;
                const char* wb = (const char*)wqkv + pn * 256 - 4096;
                int kc = (((pn + pm) & 3) * 5) >> 1;
                auto issue = [&](int c) {
                    glds4(xb + kc * 128, xo[0], xo[1], xo[2], xo[3], lds0 + (c % 5) * 32768 + hw * 4096);
                    glds4(wb + kc * 64 * (3 * D * 2), wo_[0], wo_[1], wo_[2], wo_[3], lds0 + (c % 5) * 32768 + hw * 4096 + 16384);
                    kc = kc == 9 ? 0 : kc + 1;
                };
#pragma unroll
                for (int i = 0; i < 4; ++i) {
                    const int row = (hw * 4 + i) * 8 + (lane >> 3), c = (lane & 7) ^ ((row >> 1) & 7);
                    xo[i] = (unsigned)(row * (D * 2) + c * 16 + 4096 - i * 1024);
                    wo_[i] = (unsigned)(((hw * 4 + i) * 4 + ((lane >> 2) & 3)) * (3 * D * 2) + (lane >> 4) * 64 + ((lane & 3) ^ (i & 2)) * 16 + 4096 - i * 1024);
                }
                unsigned e0; gld4sc(e0, ws, 0u);
                issue(0);
                issue(1);
                wait_vm_bar<8>();
                PIN(e0); tag = mk_tag(e0);
                issue(2);
                wait_vm_bar<8>();
                issue(3);
                wait_vm_bar<8>();
                issue(4);
                u32x4 gx, gc[4];
                unsigned gxo = WS_XS + (unsigned)(pm * 128 + hw * 32 + (lane & 31)) * 16, gco = WS_CS + (unsigned)((pn * 16 + (lane >> 4)) * 1024 + hw * 256 + (lane & 15) * 16);
                PIN(gxo); PIN(gco);
                auto gather = [&]() { gld16<0, true>(gx, ws, gxo); sfor<4>([&](auto I) { gld16<0, true>(gc[I.v], ws + I.v * 4096, gco); }); };
                auto fresh = [&]() {
                    PIN(gx); PIN(gc[0]); PIN(gc[1]); PIN(gc[2]); PIN(gc[3]);
                    unsigned d = (gx[1] ^ tag) | (gx[3] ^ tag);
#pragma unroll
                    for (int i = 0; i < 4; ++i) d |= (gc[i][1] ^ tag) | (gc[i][3] ^ tag);
                    return __all(d == 0u);
                };
                wait_vm_bar<8>(); issue(5);
                wait_vm_bar<8>(); issue(6);
                wait_vm_bar<8>(); issue(7);
                wait_vm_bar<8>(); gather(); issue(8);
                wait_vm_bar<13>(); issue(9);
                wait_vm_bar<8>();
                auto prep = [&]() {
                    float a = 0.f, b = 0.f;
#pragma unroll
                    for (int i = 0; i < 4; ++i) { a += __uint_as_float(gc[i][0]); b += __uint_as_float(gc[i][2]); }
                    a = sum_x32(sum_x16(a)); b = sum_x32(sum_x16(b));
                    const unsigned mp = split_bf16(__uint_as_float(gx[2])), pa = split_bf16(a), pb = split_bf16(b);
                    *(LAS unsigned*)(sm + L_ST + (hw * 32 + (lane & 31)) * 4) = gx[0];
                    *(LAS u32x2*)(sm + L_ST + (lane < 32 ? 512 + (hw * 32 + lane) * 8 : 2560 + (lane & 3) * 128)) = lane < 32 ? (u32x2){(mp & 0xffffu) | (mp << 16), (mp >> 16) | (mp & 0xffff0000u)} : (u32x2){0u, 0u};
                    *(LAS u32x4*)(sm + L_ST + 1536 + (hw * 32 + (lane & 15) * 2) * 8) = (u32x4){pa, pa, pb, pb};
                };
                bool done = fresh();
                wait_vm_bar<0>();
#pragma unroll 1
                for (int spins = 0; spins < SPIN_CAP && !done; ++spins) { gather(); wait_vm<0>(); done = fresh(); }
                prep();
            } else {
                unsigned e0; gld4sc(e0, ws, 0u); wait_vm_bar<0>(); PIN(e0); tag = mk_tag(e0);
                __builtin_amdgcn_s_sleep(60);
                glds_touch(ws, WS_HS + (unsigned)((bid * 4 + hw) * 16 + (lane & 15)) * 128, lds0 + 1024);
                glds_touch(ws, WS_FLAG + (unsigned)((bid * 4 + hw) * 24 + min(lane, 23)) * 128, lds0 + 1024);
                if (idx == 0) { glds_touch(ws, min((unsigned)(hw * 128 + lane) << 16, WS_END), lds0 + 1024); glds_touch(ws, min((unsigned)(hw * 128 + 64 + lane) << 16, WS_END), lds0 + 1024); }
                else {
                    glds_touch(hw == 0 ? wg : hw == 1 ? wu : hw == 2 ? wd : out, min((unsigned)lane << 16, 2211836u + (hw == 3 ? 409600u : 0u)), lds0 + 1024);
                    if (hw == 0) glds_touch(wo, min((unsigned)lane << 16, 819196u), lds0 + 1024);
                }
                wait_vm<0>();
            }
            bar_l();
        }
        publish(ws, flag_off(F_QKV, pn * 16 + pm), tag, wid, 7);
        if (wid == 6) {
            if (lane < 32) *(LAS u32x4*)(sm + (lane >> 1) * 3072 + 2816 + (lane & 1) * 16) = (lane & 1) ? (u32x4){0u, 0u, 0u, 0u} : (u32x4){ONES, ONES, ONES, ONES};
            ctl[lane] = lane == 0 ? 4u : 0u; *(LAS u32x4*)(sm + L_CTL + 256 + lane * 16) = (u32x4){0u, 0u, 0u, 0u};
        }
    }
    const int unit = u0 + (wid ^ 4);
    const char* wsrc = ws + WS_WU + (unsigned)unit * 40960;
    if (wid < 4) {
        lane = lane_id();
        const int r32 = lane & 31, hf = lane >> 5, qg = wid & 1, kh = wid >> 1, TA = qp * 8 + qg * 4, hp = head >> 1, odd = head & 1;
        const char* qb = ws + WS_Q + (unsigned)(hp * 64 + TA) * TILE;
        const char* kb = ws + WS_K + (unsigned)(hp * 64) * TILE;
        unsigned long long bad0, bad1;
        {
            const unsigned rb = lds0 + kh * 24576, cst = rb + (r32 == DH ? 2816 : 2832), va = rb + 1536 + (hf * DH + vrow(r32)) * 16;
            const char* ob = ws + WS_O + (unsigned)(TA + 2 * kh) * 40960;
            asm volatile(ATTN_ASM : "={s[54:55]}"(bad0), "={s[66:67]}"(bad1)
                         : "{s[42:43]}"(qb + 2 * kh * TILE), "{s[44:45]}"(qb + (2 - 2 * kh) * TILE), "{s[46:47]}"(kb), "{s[50:51]}"(ob), "{s[52:53]}"(ob + 40960),
                           "{v164}"(odd * 1536 + lane * 16), "{v165}"(1024 + r32 * 16 + odd * 8),
                           "{v166}"(rb + lane * 16), "{v167}"(hf ? rb + 2816 : rb + 1024 + r32 * 16 + odd * 8), "{v168}"(r32 < DH ? va : cst), "{v169}"(r32 < DH ? va + 640 : cst),
                           "{v170}"(lds0 + L_CTL), "{v171}"(lds0 + L_CTL + 256 + wid * 256 + lane * 4),
                           "{v174}"(lds0 + wid * 6144 + lane * 16), "{v175}"(lds0 + (wid ^ 2) * 6144 + lane * 16), "{v240}"(head * 1024 + lane * 16), "{v241}"(32768 + head * 256 + lane * 8)
                         : ATTN_CLOBBERS);
        }
#pragma unroll 1
        for (int b = 0; b < 2; ++b)
            if (b ? bad1 : bad0) attn_exact(ws, TA + 2 * kh + b, head, lane_id());
    } else {
        lane = lane_id();
        const int hw = wid - 4;
        const int tt = ((lane & 16) ? head * DH + DH - 1 : head * DH) >> 7;
        const unsigned pf0 = flag_off(F_QKV, (5 + (lane >> 5) * 5 + tt) * 16 + (lane & 15)),
                       pf1 = flag_off(F_QKV, (lane & 2) ? (5 + (lane >> 5) * 5 + tt) * 16 + (lane & 1) * 8 : tt * 16 + qp * 2 + (lane & 1));
        const int odd = head & 1;
        auto vsrc = [&](int c) { const int s_ = c / 40, r_ = c - s_ * 40, h_ = r_ / DH; return (unsigned)((WS_V - WS_K) + s_ * 1280 + h_ * 640 + (odd * DH + r_ - h_ * DH) * 16); };
        unsigned v0 = 4096 + odd * 1536 + lane * 16, v1 = 3072 + (lane < 32 ? 1024 + lane * 16 : vsrc(lane - 32)), v2 = 2048 + vsrc(min(lane + 32, 79));
        const char* kb = ws + WS_K + (unsigned)((head >> 1) * 64) * TILE - 4096;
        auto kv2 = [&](int t0, int n) {
#pragma unroll
            for (int k = 0; k < n; ++k) {
                const int t = t0 + (k >> 1);
                unsigned keep;
                asm volatile("s_mov_b32 %0, m0\n\ts_mov_b32 m0, %5\n\ts_nop 0\n\tglobal_load_lds_dwordx4 %1, %4 sc1\n\tglobal_load_lds_dwordx4 %2, %4 offset:1024 sc1\n\t"
                             "s_mov_b32 exec_hi, 0xffff\n\tglobal_load_lds_dwordx4 %3, %4 offset:2048 sc1\n\ts_mov_b32 exec_hi, -1\n\ts_mov_b32 m0, %0"
                             : "=&s"(keep) : "v"(v0), "v"(v1), "v"(v2), "s"(kb + ((k & 1) * 32 + t) * TILE), "s"(lds0 + (k & 1) * 24576 + (t & 7) * 3072) : "memory");
            }
        };
        PIN(v0); PIN(v1); PIN(v2);
        bool all = false;
        if (hw == 3) {
#pragma unroll 1
            for (int spins = 0; spins < SPIN_CAP; ++spins) {
                unsigned v, w; asm volatile("global_load_dword %0, %2, %4 sc1\n\tglobal_load_dword %1, %3, %4 sc1\n\ts_waitcnt vmcnt(0)" : "=&v"(v), "=&v"(w) : "v"(pf0), "v"(pf1), "s"(ws) : "memory");
                all = __all(v == tag);
                if (__all(w == tag)) break;
            }
        }
        bar_l();
        kv2(hw, 2);
        if (hw == 3 && !all) { unsigned v; gld4sc(v, ws, pf0); wait_vm<0>(); PIN(v); all = __all(v == tag); }
        wait_vm_bar<0>();
        __builtin_amdgcn_s_setprio(3);
        int wdone = 0;
        if (hw == 3) {
            if (!all) poll(ws, pf0, tag);
            int issued = 4, pub = 4;
            auto wnext = [&]() { sfor<10>([&](auto I) { if (wdone == I.v) wload4<I.v, 24>(wsrc, lane * 16); }); ++wdone; };
#pragma unroll 1
            for (int spins = 0; pub < 32 && spins < SPIN_CAP * 8; ++spins) {
                bool can = issued < 32;
                if (can) can = __all(ctl[64 + (lane & 3) * 64] + 6u >= (unsigned)issued);
                if (can) {
                    kv2(issued, 4);
                    issued += 2;
                    if (issued == 8) ctl[3] = 1u;
                    if (issued - pub > 2) { wait_vm<12>(); pub = issued - 2; ctl[0] = (unsigned)pub; }
                } else if (pub < issued) { if (wdone < 10 && uni(ctl[2]) != 0u) { wnext(); wait_vm<4>(); } else wait_vm<0>(); pub = issued; ctl[0] = pub == 32 ? 64u : (unsigned)pub; }
                else if (wdone < 10 && uni(ctl[2]) != 0u) wnext();
                else __builtin_amdgcn_s_sleep(6);
            }
        } else {
            const int it0 = bid * 8 + hw * 3, nit = min(hw == 2 ? 2 : 3, 1820 - it0), l8 = lane >> 3;
            const unsigned scr = L_WO + hw * 12288;
#pragma unroll 1
            for (int spins = 0; spins < SPIN_CAP * 8 && uni(ctl[3]) == 0u; ++spins) __builtin_amdgcn_s_sleep(2);
            if (it0 >= 1280) __builtin_amdgcn_s_sleep(60);
#pragma unroll 1
            for (int pass = 0; pass < 2; ++pass) {
#pragma unroll 1
                for (int k = 0; k < nit; ++k) {
                    const int it = it0 + k, kind = it < 200 ? 0 : it < 1280 ? 1 : 2;
                    const int B = kind == 0 ? it / 20 : kind == 1 ? (it - 200) / 40 : (it - 1280) / 54, per = kind == 0 ? 20 : kind == 1 ? 40 : 54;
                    const int ks = it - (kind == 0 ? 0 : kind == 1 ? 200 : 1280) - B * per;
                    if (pass == 0) {
                        const int pitch = kind == 1 ? FF * 2 : D * 2;
                        const bool tail = kind == 0 && ks >= 16;
                        const int r0 = kind == 1 ? (ks >> 1) * 32 : kind == 2 ? ks * 32 : tail ? (ks - 16) * 160 + 16 : ks * 40;
                        const unsigned so = (unsigned)((r0 + (tail ? (l8 >> 2) * 20 + (l8 & 3) : l8)) * pitch + B * 128 + (lane & 7) * 16);
                        const char* sb = (const char*)(kind == 0 ? wo : kind == 2 ? wd : (ks & 1) ? wu : wg);
#pragma unroll
                        for (int i = 0; i < 4; ++i) {
                            const int ri = kind ? i * 8 : tail ? i * 40 : (i >> 1) * 20 + (i & 1) * 8;
                            glds1<false>(sb + ri * pitch, so, lds0 + scr + k * 4096 + i * 1024);
                            __builtin_amdgcn_s_sleep(2);
                        }
                    } else {
                        LAS const char* p = sm + scr + k * 4096 + ((lane >> 4) * 8 + ((lane >> 2) & 3)) * 128 + (lane & 3) * 8;
#pragma unroll
                        for (int i = 0; i < 4; ++i) {
                            const int n = 4 * B + i, c5 = n / 5;
                            const unsigned dst = kind == 1 ? WS_WU + (unsigned)(n * 40 + ks) * 1024 : (kind == 0 ? WS_WO : WS_WD) + (unsigned)((c5 * per + ks) * 5 + n - c5 * 5) * 1024;
                            st16(ws + dst, lane * 16, trfrag(p + i * 32, p + i * 32 + 512));
                        }
                    }
                }
                wait_vm<0>();
            }
            if (lane == 0) { if (__hip_atomic_fetch_add((LAS unsigned*)(sm + L_CTL + 4), 1u, __ATOMIC_RELAXED, __HIP_MEMORY_SCOPE_WORKGROUP) == 2) st4(ws, flag_off(F_RT, bid), tag); }
        }
        if (hw == 0) {
            const int b0 = (ct * 5) >> 2, b1 = (ct * 5 + 4) >> 2;
            const int lo = lane < 16 ? b0 * 20 : 200 + (u0 >> 2) * 40, hi = lane < 16 ? b1 * 20 + 19 : 239 + ((u0 + 6) >> 2) * 40;
            const unsigned fo = flag_off(F_RT, lane < 32 ? min((lo >> 3) + (lane & 15), hi >> 3) : bid);
#pragma unroll 1
            for (int spins = 0; spins < SPIN_CAP; ++spins) {
                unsigned v; asm volatile("global_load_dword %0, %1, %2 sc1\n\ts_waitcnt vmcnt(0)" : "=&v"(v) : "v"(fo), "s"(ws) : "memory");
                if (__all(v == tag)) break;
                __builtin_amdgcn_s_sleep(8);
            }
            ctl[2] = 1u;
        } else {
#pragma unroll 1
            for (int spins = 0; spins < SPIN_CAP * 8 && uni(ctl[2]) == 0u; ++spins) __builtin_amdgcn_s_sleep(8);
        }
        if (hw < 3) {
            const char* src = ws + WS_WO + (unsigned)ct * 102400;
#pragma unroll 1
            for (int i = hw; i < 25; i += 3) { glds_lin<4>(src + i * 4096, lane * 16, lds0 + L_WO + i * 4096); wait_vm<4>(); }
            glds_touch(ws + WS_WU + (unsigned)(u0 + 4 + hw) * 40960 + (idx & 3) * 10240, (unsigned)lane * 128, lds0 + L_CTL + 2048);
            glds_touch(ws + WS_WU + (unsigned)(u0 + 4 + hw) * 40960 + (idx & 3) * 10240 + 8192, (unsigned)(lane & 15) * 128, lds0 + L_CTL + 2048);
        }
        sfor<10>([&](auto I) { if (I.v >= wdone) wload4<I.v, 4>(wsrc, lane * 16); });
        {
            lane = lane_id();
            const int T = (pm64 >> 1) * 4 + (pm64 & 1) + (hw & 2);
            asm volatile("global_load_dwordx2 v[84:85], %0, %1\n\tglobal_load_dwordx2 v[86:87], %0, %1 offset:32\n\tglobal_load_dwordx2 v[88:89], %0, %1 offset:64\n\t"
                         "global_load_dwordx2 v[90:91], %0, %1 offset:96\n\tglobal_load_dwordx2 v[92:93], %0, %1 offset:128"
                         :: "v"((unsigned)((T * 32 + (hw & 1) * 16 + (lane & 15)) * D + ct * 80 + (lane >> 4) * 4) * 2), "s"(x)
                         : "v84", "v85", "v86", "v87", "v88", "v89", "v90", "v91", "v92", "v93", "v94", "v95", W_CLOBBERS);
        }
        __builtin_amdgcn_s_setprio(0);
        bar(); bar();
    }
    publish(ws, flag_off(F_ATT, qp * 32 + head), tag, wid, 3);
    lane = lane_id();
    if (wid < 3) {
        sfor<3>([&](auto I) { wload4<I.v, 63>(wsrc, lane * 16); });
    }
    asm volatile("s_cmp_eq_u32 %0, 3\n\ts_cbranch_scc1 1f\n\ts_waitcnt vmcnt(0)\n\t" COLSUM_LO "s_cmp_lt_u32 %0, 3\n\ts_cbranch_scc1 1f\n\t" COLSUM_HI "1:\n\ts_nop 2"
                 :: "s"(wid) : "v84", "v85", "v86", "v87", "v88", "v89", "v90", "v91", "v92", "v93", "v94", "v95", "scc", W_CLOBBERS);
    u32x16 wq[10]; u32x2 xr[5]; float csg, csu;
    asm volatile("" : "={v[84:85]}"(xr[0]), "={v[86:87]}"(xr[1]), "={v[88:89]}"(xr[2]), "={v[90:91]}"(xr[3]), "={v[92:93]}"(xr[4]), "={v94}"(csg), "={v95}"(csu), "={v[96:111]}"(wq[0]), "={v[112:127]}"(wq[1]), "={v[128:143]}"(wq[2]), "={v[144:159]}"(wq[3]), "={v[160:175]}"(wq[4]),
                      "={v[176:191]}"(wq[5]), "={v[192:207]}"(wq[6]), "={v[208:223]}"(wq[7]), "={v[224:239]}"(wq[8]), "={v[240:255]}"(wq[9]));
    {
        const int g = lane >> 4, c16 = lane & 15, tb = wid & 3, kh = wid >> 2;
        const int T = (pm64 >> 1) * 4 + (pm64 & 1) + (tb & 2);
        unsigned hoff = WS_H + (unsigned)(T * 40 + ct * 5 + 2) * 1024 + (g >> 1) * 512 + ((tb & 1) * 16 + c16) * 16 + (g & 1) * 8;
        f32x4 acc[5] = {};
        if (kh == 1) {
#pragma unroll
            for (int n = 0; n < 5; ++n) { acc[n] = (f32x4){bf_lo(xr[n][0]), bf_hi(xr[n][0]), bf_lo(xr[n][1]), bf_hi(xr[n][1])}; }
        }
        LAS const char* wl = sm + opq_add(lane * 16, L_WO + kh * 40960);
        LAS const char* wt = sm + opq_add(lane * 16, L_WO + 81920 + kh * 10240);
        u32x4 wf[2][5];
        auto ldw = [&](int m, int n) { wf[m & 1][n] = *(LAS const u32x4*)((m < 8 ? wl + m * 5120 : wt + (m - 8) * 5120) + n * 1024); };
#pragma unroll
        for (int n = 0; n < 5; ++n) ldw(0, n);
        const char* ob = ws + WS_O + (unsigned)T * 40960;
        const char *ob0 = ob + kh * 16384 + 4096, *ob1 = ob0 + 8192;
        unsigned ao = (g >> 1) * 1024 + (g & 1) * 512 + ((tb & 1) * 16 + c16) * 16, to = 32768 + kh * 4096 + g * 512 + ((tb & 1) * 16 + c16) * 8;
        unsigned so = WS_HS + (unsigned)((T * 2 + (tb & 1)) * 8 + ct) * 128 + lane * 8; int ro = tb * 5120 + lane * 16;
        PIN(ao); PIN(to); PIN(hoff); PIN(so); PIN(ro);
        asm volatile("" : "+s"(ob0), "+s"(ob1), "+s"(ob));
#pragma unroll
        for (int n = 0; n < 5; ++n) PIN(acc[n]);
        if (wid == 3) poll(ws, flag_off(F_ATT, xcd * 32 + (lane & 31)), tag);
        bar();
        u32x4 af[8]; u32x2 at[4];
        auto lda = [&](auto M) {
            constexpr int m = M.v;
            if constexpr (m < 8) gld16<(m & 3) * 2048 - 4096, true>(af[m], m < 4 ? ob0 : ob1, ao);
            else if constexpr (m < 10) { gld8<(m - 8) * 2048, true>(at[2 * (m - 8)], ob, to); gld8<(m - 8) * 2048 + 256, true>(at[2 * (m - 8) + 1], ob, to); }
        };
        constexpr int LA = 4;
        sfor<LA>([&](auto I) { lda(I); });
        sfor<10>([&](auto M) {
            constexpr int m = M.v;
            constexpr int last = m + LA - 1 < 9 ? m + LA - 1 : 9;
            constexpr int young = (last - m) + (last >= 8 ? (last - (m > 7 ? m : 7)) : 0);
            wait_vm<young>();
            u32x4 a;
            if constexpr (m < 8) { PIN(af[m]); a = af[m]; }
            else { PIN(at[2 * (m - 8)]); PIN(at[2 * (m - 8) + 1]); a = (u32x4){at[2 * (m - 8)][0], at[2 * (m - 8)][1], at[2 * (m - 8) + 1][0], at[2 * (m - 8) + 1][1]}; }
#pragma unroll
            for (int n = 0; n < 5; ++n) { const u32x4 w = wf[m & 1][n]; if (m < 9) ldw(m + 1, n); acc[n] = mfma16(w, a, acc[n]); SBAR(); }
            lda(IC<m + LA>{});
        });
        LAS f32x4* red = (LAS f32x4*)(sm + ro);
        if (kh == 0) {
#pragma unroll
            for (int n = 0; n < 5; ++n) red[n * 64] = acc[n];
        }
        bar_l();
        if (kh == 1) {
            f32x4 o[5];
#pragma unroll
            for (int n = 0; n < 5; ++n) o[n] = red[n * 64];
            float s1 = 0.f, s2 = 0.f, s3 = 0.f, s4 = 0.f;
            sfor<5>([&](auto N) {
                const f32x4 t = acc[N.v] + o[N.v];
                const u32x2 hv = {pk2(t[0], t[1]), pk2(t[2], t[3])};
                asm volatile("global_store_dwordx2 %0, %1, %2 offset:%3 sc1" :: "v"(hoff), "v"(hv), "s"(ws), "i"(N.v * 1024 - 2048) : "memory");
                s1 = dot2(hv[0], ONES, s1); s2 = dot2(hv[0], hv[0], s2); s3 = dot2(hv[1], ONES, s3); s4 = dot2(hv[1], hv[1], s4);
                SBAR();
            });
            s1 = sum_x32(sum_x16(s1 + s3)); s2 = sum_x32(sum_x16(s2 + s4));
            asm volatile("s_mov_b64 exec, 0xffff\n\tglobal_store_dwordx2 %0, %1, %2 sc1\n\ts_mov_b64 exec, -1" :: "v"(so), "v"((u32x2){__float_as_uint(s1), __float_as_uint(s2)}), "s"(ws) : "memory");
        }
        publish(ws, flag_off(F_WO, (pm64 >> 1) * 16 + (pm64 & 1) * 8 + ct), tag, wid, 3);
    }
    constexpr int L_SP = 122880, L_S2 = L_SP + 32768, L_GO = 163824;
    constexpr unsigned NOT_YET = 0x7fc5a5a5u;
    const char* wdb = ws + WS_WD + (unsigned)ct * 54 * 5120;
    {
        {
            const char* s5 = wsrc + 3 * 4096;
            asm volatile("s_cmp_gt_u32 %12, 2\n\ts_cbranch_scc1 1f\n\t"
#define WL4(r) "global_load_dwordx4 v[" #r ":" #r "+3], %11, s[40:41] sc1\n\tglobal_load_dwordx4 v[" #r "+4:" #r "+7], %11, s[40:41] offset:1024 sc1\n\t" \
               "global_load_dwordx4 v[" #r "+8:" #r "+11], %11, s[40:41] offset:2048 sc1\n\tglobal_load_dwordx4 v[" #r "+12:" #r "+15], %11, s[40:41] offset:3072 sc1\n\t" \
               "s_add_u32 s40, s40, 4096\n\ts_addc_u32 s41, s41, 0\n\t"
                         WL4(144) WL4(160) WL4(176) WL4(192) WL4(208) WL4(224) WL4(240) "1:"
                         : W_INOUT, "+{s[40:41]}"(s5) : "v"(lane * 16), "s"(wid) : "scc", "memory");
        }
        const char* hb = ws + WS_H + (unsigned)pm128 * 4 * 40960 + wid * 5120;
        auto five = [&](const char* src, int slot) { glds_lin<4>(src, lane * 16, lds0 + slot * 40960 + wid * 5120); glds_lin<1>(src + 4096, lane * 16, lds0 + slot * 40960 + wid * 5120 + 4096); };
        const int TE = (pm64 >> 1) * 4 + (pm64 & 1);
        const char* gb = ws + WS_G + (unsigned)TE * 108 * 1024;
        const char* h0 = ws + WS_H + (unsigned)pm128 * 4 * 40960 + wid * 1024;
        unsigned l16 = lane * 16; PIN(l16);
        asm volatile("" : "+s"(h0));
        if (wid == 3) poll(ws, (lane & 16) ? flag_off(F_RT, min(((1280 + ((ct * 5) >> 2) * 54) >> 3) + (lane & 15), (1333 + ((ct * 5 + 4) >> 2) * 54) >> 3))
                                             : flag_off(F_WO, pm128 * 16 + (lane & 15)), tag);
        bar();
        sfor<5>([&](auto I) { glds_lin<1>(h0 + I.v * 8192, l16, lds0 + wid * 1024 + I.v * 8192); });
        if (wid != 3) {
            asm volatile("s_cmp_gt_u32 %12, 2\n\ts_cbranch_scc1 1f\n\ts_waitcnt vmcnt(5)\n\t" COLSUM_HI "s_nop 2\n\t1:" : "+{v94}"(csg), "+{v95}"(csu), W_INOUT : "s"(wid) : "scc");
            const unsigned cg = split_bf16(csg), cu = split_bf16(csu);
            const int rk = (wid - (wid > 3) + ct) % 7, nb = wid & 3;
            {
                char* gp = ws; const char* wsr = wdb + (21 + 4 * rk) * 5120 + 4096;
                asm volatile(UP_ASM : "+{s[44:45]}"(gp), "+{s[56:57]}"(wsr),
                               "+{v[96:111]}"(wq[0]), "+{v[112:127]}"(wq[1]), "+{v[128:143]}"(wq[2]), "+{v[144:159]}"(wq[3]), "+{v[160:175]}"(wq[4])
                             : "{s[40:41]}"(hb), "{s43}"(lds0 + wid * 5120), "{s[54:55]}"(wdb + 6 * 5120 + wid * 5120), "{v[78:79]}"((u32x2){cg, cg}), "{v[80:81]}"((u32x2){cu, cu}),
                               "{v89}"(lds0 + (lane >> 5) * 1024 + ((lane >> 4) & 1) * 512 + (lane & 15) * 16), "{v91}"(lane * 16),
                               "{v92}"(WS_G + (unsigned)(pm128 * 4 * 108 + unit) * 1024 + lane * 16), "{v93}"(lds0 + L_S2 + (lane & 15) * 32),
                               "{v[176:191]}"(wq[5]), "{v[192:207]}"(wq[6]), "{v[208:223]}"(wq[7]), "{v[224:239]}"(wq[8]), "{v[240:255]}"(wq[9])
                             : "v0", "v1", "v2", "v3", "v4", "v5", "v6", "v7", "v8", "v9", VCL(1), VCL(2), VCL(3), VCL(4), VCL(5), VCL(6), "v70", "v71", "v72", "v73", "v74", "v75", "v76", "v77",
                               "v82", "v83", "v84", "v85", "v86", "v87", "v88", "v90", "v94", "s47", "s48", "s49", "scc", "memory");
            }
            lane = lane_id();
            const int TR = wid < 3 ? TE : TE + 2;
            const unsigned ae = (lane >> 5) * 1024 + ((lane >> 4) & 1) * 512 + (lane & 15) * 16;
            unsigned oo = (unsigned)((TR * 32 + (lane & 31)) * D + ct * 80 + nb * 16 + (lane >> 5) * 8) * 2, lr = lds0 + nb * 1024 + lane * 16;
            asm volatile(DOWN_ASM : "+{v93}"(oo), "+{v92}"(lr)
                         : "{s[42:43]}"(gb + (3 * rk + 1) * 2048), "{s[44:45]}"(gb + (23 + 4 * rk) * 2048), "{s46}"(wid), "{s[48:49]}"(out),
                           "{s[50:51]}"(ws + WS_H + (unsigned)(TR * 40 + ct * 5 + nb) * 1024), "{s[52:53]}"(ws + WS_H + (unsigned)((TE + 2) * 40 + ct * 5 + 4) * 1024), "{s54}"(NOT_YET),
                           "{v88}"(lane * 16), "{v89}"(ae), "{v90}"(ae + 2 * 108 * 1024), "{v91}"(lds0 + (rk < 2 ? L_SP + rk * 15360 : 10240 + rk * 15360) + lane * 16),
                           "{v94}"(lds0 + rk * 5120 + lane * 16), "{v95}"(lds0 + L_GO),
                           "{v[96:111]}"(wq[0]), "{v[112:127]}"(wq[1]), "{v[128:143]}"(wq[2]), "{v[144:159]}"(wq[3]), "{v[160:175]}"(wq[4])
                         : "v0", "v1", "v2", "v3", "v4", "v5", "v6", "v7", "v8", "v9", VCL(1), VCL(2), VCL(3), VCL(4), VCL(5), VCL(6), VCL(7), "v80", "v81", "v82", "v83", "v84", "v85", "v86", "v87",
                           "v176", "v177", "v178", "v179", VCL(18), VCL(19), VCL(20), VCL(21), VCL(22), VCL(23), VCL(24),
                           "v250", "v251", "v252", "v253", "v254", "v255", "s47", "vcc", "scc", "memory");
        } else {
            lane = lane_id();
            u32x4 hs[8];
            sfor<8>([&](auto I) { gld16<I.v * 128, true>(hs[I.v], ws, WS_HS + (unsigned)(pm128 * 8 + (lane >> 3)) * 1024 + (lane & 7) * 16); });
            wait_vm_bar<11>();
            wait_vm_bar<8>();
            five(hb + 40960, 1);
            wait_vm<5>();
            float a[4] = {0.f, 0.f, 0.f, 0.f};
#pragma unroll
            for (int i = 0; i < 8; ++i) { PIN(hs[i]); a[0] += __uint_as_float(hs[i][0]); a[1] += __uint_as_float(hs[i][1]); a[2] += __uint_as_float(hs[i][2]); a[3] += __uint_as_float(hs[i][3]); }
#pragma unroll
            for (int i = 0; i < 2; ++i) {
                const float mean = a[2 * i] * (1.f / D), var = fmaxf(a[2 * i + 1] * (1.f / D) - mean * mean, 0.f) + LN_EPS, rstd = __builtin_amdgcn_rsqf(var);
                const float k1 = -2.f * 0.7978845608028654f * 1.4426950408889634f * rstd, k2 = k1 * 0.044715f * rstd * rstd;
                const unsigned mp = split_bf16(-mean);
                *(LAS f32x4*)(sm + L_S2 + (2 * lane + i) * 32) = (f32x4){k1, k2, var, 0.f};
                *(LAS u32x2*)(sm + L_S2 + (2 * lane + i) * 32 + 16) = (u32x2){(mp & 0xffffu) | (mp << 16), (mp >> 16) | (mp & 0xffff0000u)};
            }
            LAS volatile unsigned* go = (LAS volatile unsigned*)(sm + L_GO);
            go[lane & 3] = NOT_YET;
            asm volatile("s_waitcnt vmcnt(0) lgkmcnt(0)\n\ts_barrier" ::: "memory");
            five(hb + 2 * 40960, 2);
            sfor<8>([&](auto I) { glds_lin<I.v < 7 ? 4 : 2>(wdb + I.v * 4096, lane * 16, lds0 + L_SP + I.v * 4096); });
            wait_vm_bar<30>();
            five(hb + 3 * 40960, 0);
            wait_vm<25>();
            u32x4 w5[25];
            sfor<25>([&](auto I) { gld16<(I.v & 3) * 1024, true>(w5[I.v], wdb + 49 * 5120 + (I.v >> 2) * 4096, lane * 16); });
            wait_vm_bar<25>();
            five(wdb + 6 * 5120 + wid * 5120, 1);
            unsigned fa = flag_off(F_UPE, pm128 * 16 + cj), fb = flag_off(F_UP, pm128 * 16 + cj), tv = tag; PIN(fa); PIN(fb); PIN(tv);
            bar();
            if (lane == 0) st4(ws, fa, tv);
            five(wdb + 14 * 5120 + wid * 5120, 2);
            const unsigned fe = flag_off(F_UPE, (pm64 >> 1) * 16 + (lane & 15));
            const unsigned ae = (lane >> 5) * 1024 + ((lane >> 4) & 1) * 512 + (lane & 15) * 16;
            f32x4 acc[10];
            auto part = [&](u32x4 (&gf)[10]) {
#pragma unroll
                for (int t = 0; t < 10; ++t) acc[t] = (f32x4){0.f, 0.f, 0.f, 0.f};
                sfor<10>([&](auto I) { PIN(gf[I.v]); _Pragma("unroll") for (int n = 0; n < 5; ++n) acc[(I.v & 1) * 5 + n] = mfma16(w5[(I.v >> 1) * 5 + n], gf[I.v], acc[(I.v & 1) * 5 + n]); });
            };
            auto dump = [&](int p0, int p1) { _Pragma("unroll") for (int t = 0; t < 10; ++t) *(LAS f32x4*)(sm + opq_add(lane * 16, (t < 5 ? p0 : p1) * 5120) + (t % 5) * 1024) = acc[t]; };
            bool eok = false;
#pragma unroll 1
            for (int tries = 0; tries < 3 && !eok; ++tries) { unsigned fv; gld4sc(fv, ws, fe); wait_vm<0>(); PIN(fv); eok = __all(fv == tag); }
#pragma unroll
            for (int i = 0; i < 25; ++i) PIN(w5[i]);
            if (eok) go[0] = 1u;
            bar_l();
            if (lane == 0) st4(ws, fb, tv);
            __builtin_amdgcn_s_setprio(3);
            if (!eok) { poll(ws, fe, tag); go[0] = 1u; }
            u32x4 ge[10];
            sfor<10>([&](auto I) { gld16<(I.v & 1) * 256, true>(ge[I.v], gb + (49 + (I.v >> 1)) * 2048, ae); });
            poll(ws, flag_off(F_UP, (pm64 >> 1) * 16 + (lane & 15)), tag); go[1] = 1u;
            u32x4 gl[10];
            sfor<10>([&](auto I) { gld16<(I.v & 1) * 256, true>(gl[I.v], gb + 2 * 108 * 1024 + (49 + (I.v >> 1)) * 2048, ae); });
            part(ge); dump(7, 23);
            asm volatile("s_waitcnt lgkmcnt(0)" ::: "memory");
            if (lane == 0) __hip_atomic_fetch_add((LAS unsigned*)(sm + L_GO + 8), 1u, __ATOMIC_RELAXED, __HIP_MEMORY_SCOPE_WORKGROUP);
            wait_vm<0>();
            part(gl);
#pragma unroll 1
            for (int spins = 0; spins < SPIN_CAP * 16 && uni(go[2]) - NOT_YET < 8u; ++spins) {}
            dump(30, 31);
            bar_l();
            asm volatile(".Lcode_hi:");
            if (bid == 0 && lane == 0) st4(ws, 0u, tag);
        }
    }
}

extern "C" void kernel_launch(void* const* d_in, const int* in_sizes, int n_in, void* d_out, int out_size, void* d_ws, size_t ws_size, hipStream_t stream) {
    (void)hipFuncSetAttribute((const void*)block_kernel, hipFuncAttributeMaxDynamicSharedMemorySize, 163840);
    block_kernel<<<256, 512, 163840, stream>>>((const u16*)d_in[0], (const u16*)d_in[1], (const u16*)d_in[2], (const u16*)d_in[4], (const u16*)d_in[5], (const u16*)d_in[6], (u16*)d_out, (char*)d_ws);
}
```
